```python
import torch

def _softmax(x, axis=-1):
    return torch.softmax(x, dim=axis)

def _gelu(x):
    return torch.nn.functional.gelu(x, approximate='tanh')

def _silu(x):
    return torch.nn.functional.silu(x)

def _sigmoid(x):
    return torch.sigmoid(x)

def _layernorm(x, axis=-1, eps=1e-05):
    m = x.mean(dim=axis, keepdim=True)
    v = x.var(dim=axis, keepdim=True, unbiased=False)
    return (x - m) / torch.sqrt(v + eps)

def _scatter_add(tgt, idx, src, axis=0):
    return tgt.clone().index_add_(axis, idx, src)

def _segment_sum(src, seg, n):
    out = torch.zeros((n,) + src.shape[1:], dtype=src.dtype)
    return out.index_add_(0, seg, src)

def _pad(x, pw):
    flat = tuple((v for pair in reversed(pw) for v in pair))
    return torch.nn.functional.pad(x, flat)
kernel_name = 'adaLN_cond__sliding_block_cached__moe_arch_mod__decode'

def setup_inputs():

    _w = lambda a: (lambda t: t.to(torch.bfloat16) if t.is_floating_point() else t)(torch.as_tensor(a))

    def _A_setup():
        B, Sq, D = (8, 1, 576)
        Hq, Hkv, Sk, Dh = (32, 16, 8192, 128)
        assert Hq % Hkv == 0, (Hq, Hkv)
        return {'x': _w((lambda *s: torch.randn(*s, dtype=torch.bfloat16) * 0.1)(B, Sq, D)), 'k_cache': _w((lambda *s: torch.randn(*s, dtype=torch.bfloat16) * 0.8)(B, Hkv, Sk, Dh)), 'v_cache': _w((lambda *s: torch.randn(*s, dtype=torch.bfloat16) * 0.8)(B, Hkv, Sk, Dh)), 'wq': _w((lambda *s: torch.randn(*s, dtype=torch.bfloat16) * 0.8)(D, Hq * Dh) * D ** (-0.5)), 'wo': _w((lambda *s: torch.randn(*s, dtype=torch.bfloat16) * 0.8)(Hq * Dh, D) * (Hq * Dh) ** (-0.5))}
    _A_in = _A_setup()

    def _B_setup():
        B, S, D, F, C = (8, 1, 576, 4608, 1)
        return {'x': _w((lambda *s: torch.randn(*s, dtype=torch.bfloat16) * 0.1)(B, S, D)), 'w_r': _w((lambda *s: torch.randn(*s, dtype=torch.bfloat16) * 0.2)(D)), 'w_gate': _w((lambda *s: torch.randn(*s, dtype=torch.bfloat16) * 0.1)(D, F)), 'w_up': _w((lambda *s: torch.randn(*s, dtype=torch.bfloat16) * 0.1)(D, F)), 'w_down': _w((lambda *s: torch.randn(*s, dtype=torch.bfloat16) * 0.1)(F, D))}
    _B_in = _B_setup()
    return {'A_x': _A_in['x'], 'A_k_cache': _A_in['k_cache'], 'A_v_cache': _A_in['v_cache'], 'A_wq': _A_in['wq'], 'A_wo': _A_in['wo'], 'B_x': _B_in['x'], 'B_w_r': _B_in['w_r'], 'B_w_gate': _B_in['w_gate'], 'B_w_up': _B_in['w_up'], 'B_w_down': _B_in['w_down'], 'c': _w((lambda *s: torch.randn(*s, dtype=torch.bfloat16) * 0.4)(8, 576) * 8.0), 'w_mod': _w((lambda *s: torch.randn(*s, dtype=torch.bfloat16) * 0.4)(576, 6 * 576) * 576 ** (-0.5) * 8.0)}

def reference(A_x, A_k_cache, A_v_cache, A_wq, A_wo, B_x, B_w_r, B_w_gate, B_w_up, B_w_down, c, w_mod):

    def _A_ref(x, k_cache, v_cache, wq, wo):
        B, Sq, D = x.shape
        Hq, Dh = (32, 128)
        Hkv, Sk = (k_cache.shape[1], k_cache.shape[2])
        W = min(4096, max(256, Sk // 4))
        rep = Hq // Hkv
        q = (lambda x, *d: x.permute(*d))((x @ wq).reshape(B, Sq, Hq, Dh), 0, 2, 1, 3)
        k = (lambda x, n, axis: torch.repeat_interleave(x, n, dim=axis))(k_cache[:, :, Sk - W:, :], rep, axis=1)
        v = (lambda x, n, axis: torch.repeat_interleave(x, n, dim=axis))(v_cache[:, :, Sk - W:, :], rep, axis=1)
        scores = torch.einsum('bhqd,bhkd->bhqk', q, k) * Dh ** (-0.5)
        p = _softmax(scores, axis=-1)
        o = torch.einsum('bhqk,bhkd->bhqd', p, v)
        return (lambda x, *d: x.permute(*d))(o, 0, 2, 1, 3).reshape(B, Sq, Hq * Dh) @ wo

    def _B_ref(x, w_r, w_gate, w_up, w_down):
        B, S, D = x.shape
        C = 1
        r = x @ w_r
        idx = torch.argsort(-r, axis=-1)[:, :C]
        idx_e = (lambda x, n, axis: torch.repeat_interleave(x, n, dim=axis))(idx.reshape(B, C, 1), D, axis=2)
        x_sel = (lambda x, i, axis: torch.take_along_dim(x, i, dim=axis))(x, idx_e, axis=1)
        g = _silu(x_sel @ w_gate)
        u = x_sel @ w_up
        y = g * u @ w_down
        r_sel = _sigmoid((lambda x, i, axis: torch.take_along_dim(x, i, dim=axis))(r, idx, axis=1))
        y = y * r_sel.reshape(B, C, 1)
        b_off = (torch.arange(B) * S).reshape(B, 1)
        flat_idx = (idx + b_off).reshape(B * C)
        zero = (x * 0.0).reshape(B * S, D)
        upd = _scatter_add(zero, flat_idx, y.reshape(B * C, D), axis=0)
        return x + upd.reshape(B, S, D)
    x = A_x
    mod = (_silu(c) @ w_mod).reshape(c.shape[0], 6, 1, -1)
    sa, ca, ga, sb, cb, gb = (mod[:, 0].clone(), mod[:, 1].clone(), mod[:, 2].clone(), mod[:, 3].clone(), mod[:, 4].clone(), mod[:, 5].clone())
    h = _layernorm(x, axis=-1) * (1 + ca) + sa
    x = x + ga * _A_ref(h, A_k_cache, A_v_cache, A_wq, A_wo)
    h = _layernorm(x, axis=-1) * (1 + cb) + sb
    x = x + gb * _B_ref(h, B_w_r, B_w_gate, B_w_up, B_w_down)
    return x
```

```cpp
#define PROBE 99
#include <hip/hip_runtime.h>
#include <hip/hip_bf16.h>
#include <cstdint>

typedef unsigned u32;
typedef unsigned short u16;
typedef u32 u32x4 __attribute__((ext_vector_type(4)));
typedef u32 u32x2 __attribute__((ext_vector_type(2)));
typedef float f32x4 __attribute__((ext_vector_type(4)));
typedef float f32x2 __attribute__((ext_vector_type(2)));
typedef short s16x4 __attribute__((ext_vector_type(4)));
typedef __bf16 bf16x8 __attribute__((ext_vector_type(8)));
typedef __bf16 bf16x2 __attribute__((ext_vector_type(2)));
typedef __attribute__((address_space(3))) unsigned char lds_t;

constexpr int D = 576, SK = 8192, WIN = 2048, FF = 4608, NQ = 4096, NMOD = 6 * D;
constexpr int NWG = 256, NT = 512, NG = 72;
constexpr int SPIN_CAP = 1 << 15;

constexpr int W_O = 0;
constexpr int W_XP = W_O + 262144;
constexpr int W_X1 = W_XP + 147456;
constexpr int W_M = W_X1 + 73728;
constexpr int W_MS = W_M + 36864;
constexpr int W_Y = W_MS;
constexpr int W_FL = W_Y + 144 * NG * 128;
constexpr int W_FY = W_FL + 1024;
constexpr int W_H = W_FY + 1024;
constexpr int W_Q = W_H + 36864;
constexpr int W_ST = W_Q + 131072;
constexpr int W_TS = W_ST + 32768;
constexpr int W_END = W_TS + 72 * 128;
constexpr int PZ = (W_MS / 16 + NG - 1) / NG;
constexpr int NPY = 106, PY = (144 * NG * 8 + NPY - 1) / NPY;
static_assert(PZ <= NT && PY <= 3 * 384, "poison split");

constexpr int L_W = 0;
constexpr int L_IMG = 73728;
constexpr int L_TILE = L_IMG + 9216;
constexpr int L_OI = 65536;
constexpr int L_B = 147456;
constexpr int L_C = 155648;
constexpr int LDS_BYTES = 163840;

#define STAMP(i) do { if ((i) == PROBE) probe_ = (u32)__builtin_amdgcn_s_memrealtime() - tq0_; } while (0)
#define STAMPW(i) STAMP(i)

#define SCHED() __builtin_amdgcn_sched_barrier(0)
#define UNTIL(t) do { _Pragma("nounroll") while ((int)((u32)__builtin_amdgcn_s_memrealtime() - (t)) < 0) __builtin_amdgcn_s_sleep(1); asm volatile("" ::: "memory"); } while (0)
#define BAR() asm volatile("s_barrier" ::: "memory")
#define TS(i) do {} while (0)
#define TSOUT() do {} while (0)
#define TSA(i) do {} while (0)
#define TSB(i) do {} while (0)
#define TSC(i) do {} while (0)
#define WAITV_(n) do { asm volatile("s_waitcnt vmcnt(" #n ")" ::: "memory"); SCHED(); } while (0)
#define WAITV(n) WAITV_(n)
#define DK 5
#define DV DK
#define PR 117
#define PR2 114
#define TADJ -10
#define SLOWJ 6
#define TGO 200

__device__ __forceinline__ float lo16(u32 u) { return __uint_as_float(u << 16); }
__device__ __forceinline__ float hi16(u32 u) { return __uint_as_float(u & 0xffff0000u); }
__device__ __forceinline__ float b2f(u16 u) { return __uint_as_float((u32)u << 16); }
__device__ __forceinline__ u32 cvt2(float a, float b) { return __builtin_bit_cast(u32, __builtin_convertvector(f32x2{a, b}, bf16x2)); }
__device__ __forceinline__ float rbf(float v) { return __uint_as_float(cvt2(0.f, v)); }
__device__ __forceinline__ u32 sanf(float f) { u32 v = __float_as_uint(f); return v < 0xfffffffeu ? v : 0xfffffffeu; }
__device__ __forceinline__ u32 sanu(u32 v) { return v < 0xfffffffeu ? v : 0xfffffffeu; }
__device__ __forceinline__ f32x4 asf4(u32x4 v) { return __builtin_bit_cast(f32x4, v); }
__device__ __forceinline__ f32x2 up2(u32 u) { return f32x2{lo16(u), hi16(u)}; }
__device__ __forceinline__ f32x2 rbf2(f32x2 v) { return f32x2{rbf(v.x), rbf(v.y)}; }
__device__ __forceinline__ void lds_barrier() { asm volatile("s_waitcnt lgkmcnt(0)\n\ts_barrier" ::: "memory"); }
__device__ __forceinline__ f32x4 mfma(u32x4 a, u32x4 b, f32x4 c) {
  return __builtin_amdgcn_mfma_f32_16x16x32_bf16(__builtin_bit_cast(bf16x8, a), __builtin_bit_cast(bf16x8, b), c, 0, 0, 0);
}
__device__ __forceinline__ u32 plo(u32 a, u32 b) { return __builtin_amdgcn_perm(b, a, 0x05040100u); }
__device__ __forceinline__ u32 phi(u32 a, u32 b) { return __builtin_amdgcn_perm(b, a, 0x07060302u); }

extern "C" __device__ __attribute__((const)) unsigned long long llvm_dispatch_id() __asm("llvm.amdgcn.dispatch.id");
typedef __amdgpu_buffer_rsrc_t rsrc_t;
__device__ __forceinline__ u32x4 ld128(rsrc_t rs, int off) { return __builtin_amdgcn_raw_buffer_load_b128(rs, off, 0, 16); }
__device__ __forceinline__ u32 ld32(rsrc_t rs, int off) { return __builtin_amdgcn_raw_buffer_load_b32(rs, off, 0, 16); }
__device__ __forceinline__ void st128(rsrc_t rs, int off, u32x4 v) { __builtin_amdgcn_raw_buffer_store_b128(v, rs, off, 0, 16); }
__device__ __forceinline__ void st64(rsrc_t rs, int off, u32x2 v) { __builtin_amdgcn_raw_buffer_store_b64(v, rs, off, 0, 16); }
__device__ __forceinline__ void dma(const void* gbase, u32 voff, unsigned lds_dst) {
  unsigned keep;
  asm volatile("s_mov_b32 %0, m0\n\ts_mov_b32 m0, %3\n\ts_nop 2\n\tglobal_load_lds_dwordx4 %1, %2 nt\n\ts_mov_b32 m0, %0"
               : "=&s"(keep) : "v"(voff), "s"(gbase), "s"(lds_dst) : "memory");
}
__device__ __forceinline__ void dma_keep(const void* gbase, u32 voff, unsigned lds_dst) {
  unsigned keep;
  asm volatile("s_mov_b32 %0, m0\n\ts_mov_b32 m0, %3\n\ts_nop 2\n\tglobal_load_lds_dword %1, %2\n\ts_mov_b32 m0, %0"
               : "=&s"(keep) : "v"(voff), "s"(gbase), "s"(lds_dst) : "memory");
}
__device__ __forceinline__ void dma_sc(const void* gbase, u32 voff, unsigned lds_dst) {
  unsigned keep;
  asm volatile("s_mov_b32 %0, m0\n\ts_mov_b32 m0, %3\n\ts_nop 2\n\tglobal_load_lds_dwordx4 %1, %2 sc1\n\ts_mov_b32 m0, %0"
               : "=&s"(keep) : "v"(voff), "s"(gbase), "s"(lds_dst) : "memory");
}
__device__ __forceinline__ void ald128(u32x4& d, const void* gbase, u32 voff) { unsigned long long t; asm volatile("s_mov_b64 %1, %3\n\tglobal_load_dwordx4 %0, %2, %1" : "=v"(d), "=&s"(t) : "v"(voff), "s"(gbase) : "memory"); }
__device__ __forceinline__ void ald64(u32x2& d, const void* gbase, u32 voff) { unsigned long long t; asm volatile("s_mov_b64 %1, %3\n\tglobal_load_dwordx2 %0, %2, %1" : "=v"(d), "=&s"(t) : "v"(voff), "s"(gbase) : "memory"); }
__device__ __forceinline__ void ald128sc(u32x4& d, const void* gbase, u32 voff) { unsigned long long t; asm volatile("s_mov_b64 %1, %3\n\tglobal_load_dwordx4 %0, %2, %1 sc1" : "=v"(d), "=&s"(t) : "v"(voff), "s"(gbase) : "memory"); }
__device__ __forceinline__ u32x2 trd(const unsigned char* lds, int off) {
  return __builtin_bit_cast(u32x2, __builtin_amdgcn_ds_read_tr16_b64_v4i16((__attribute__((address_space(3))) s16x4*)((lds_t*)lds + off)));
}
#define nap(n) do { __builtin_amdgcn_s_sleep(n); asm volatile("" ::: "memory"); } while (0)
__device__ __forceinline__ float frcp(float v) { return __builtin_amdgcn_rcpf(v); }
__device__ __forceinline__ float fdiv(float a, float d, float r) { const float q = a * r; return fmaf(fmaf(-q, d, a), r, q); }
__device__ __forceinline__ float silu(float v) { const float d = 1.f + __expf(-v); const float r = frcp(d); return d < 3.0e38f ? fdiv(v, d, r) : v * r; }
template <int CTRL> __device__ __forceinline__ float dppf(float x) {
  return __builtin_bit_cast(float, __builtin_amdgcn_mov_dpp(__builtin_bit_cast(int, x), CTRL, 0xf, 0xf, true));
}
template <int CTRL> __device__ __forceinline__ u32 dppu(u32 x) { return (u32)__builtin_amdgcn_mov_dpp((int)x, CTRL, 0xf, 0xf, true); }
__device__ __forceinline__ u32 mxu(u32 a, u32 b) { return a > b ? a : b; }
__device__ __forceinline__ u32 mx4(u32x4 v) { return mxu(mxu(v[0], v[1]), mxu(v[2], v[3])); }
__device__ __forceinline__ float rl(float v, int l) { return __builtin_bit_cast(float, __builtin_amdgcn_readlane(__builtin_bit_cast(int, v), l)); }
__device__ __forceinline__ float wsum(float v) {
  v += dppf<0xB1>(v); v += dppf<0x4E>(v); v += dppf<0x141>(v); v += dppf<0x140>(v);
  return (rl(v, 0) + rl(v, 16)) + (rl(v, 32) + rl(v, 48));
}
__device__ __forceinline__ float up32(float v) { const u32 u = __float_as_uint(v); return __uint_as_float(__builtin_amdgcn_permlane32_swap(u, u, false, false)[1]); }
__device__ __forceinline__ float xsum32(float v) { const u32 u = __float_as_uint(v); const auto r = __builtin_amdgcn_permlane32_swap(u, u, false, false); return __uint_as_float(r[0]) + __uint_as_float(r[1]); }
__device__ __forceinline__ float xsum16(float v) { const u32 u = __float_as_uint(v); const auto r = __builtin_amdgcn_permlane16_swap(u, u, false, false); return __uint_as_float(r[0]) + __uint_as_float(r[1]); }
__device__ __forceinline__ float xmax32(float v) { const u32 u = __float_as_uint(v); const auto r = __builtin_amdgcn_permlane32_swap(u, u, false, false); return fmaxf(__uint_as_float(r[0]), __uint_as_float(r[1])); }
__device__ __forceinline__ float xmax16(float v) { const u32 u = __float_as_uint(v); const auto r = __builtin_amdgcn_permlane16_swap(u, u, false, false); return fmaxf(__uint_as_float(r[0]), __uint_as_float(r[1])); }
__device__ __forceinline__ float red8(f32x4 v) {
  const auto a = __builtin_amdgcn_permlane32_swap(__float_as_uint(v[0]), __float_as_uint(v[2]), false, false);
  const auto b = __builtin_amdgcn_permlane32_swap(__float_as_uint(v[1]), __float_as_uint(v[3]), false, false);
  const float w0 = __uint_as_float(a[0]) + __uint_as_float(a[1]), w1 = __uint_as_float(b[0]) + __uint_as_float(b[1]);
  const auto c = __builtin_amdgcn_permlane16_swap(__float_as_uint(w0), __float_as_uint(w1), false, false);
  const float z = __uint_as_float(c[0]) + __uint_as_float(c[1]);
  return z + dppf<0x128>(z);
}
__device__ __forceinline__ u32x2 gather4(float z) {
  const u32 u = __float_as_uint(z);
  const auto a = __builtin_amdgcn_permlane16_swap(u, u, false, false);
  const u32 pk = phi(a[0], a[1]);
  const auto b = __builtin_amdgcn_permlane32_swap(pk, pk, false, false);
  return u32x2{b[0], b[1]};
}
__device__ __forceinline__ f32x4 half_sum(f32x4 d) {
  f32x4 r;
#pragma unroll
  for (int j = 0; j < 4; ++j) r[j] = d[j] + dppf<0x108>(up32(d[j]));
  return r;
}
__device__ __forceinline__ float half_sum1(f32x4 d, bool lo) {
  const auto a = __builtin_amdgcn_permlane32_swap(__float_as_uint(d[0]), __float_as_uint(d[2]), false, false);
  const auto b = __builtin_amdgcn_permlane32_swap(__float_as_uint(d[1]), __float_as_uint(d[3]), false, false);
  return __uint_as_float(lo ? a[0] : b[1]) + dppf<0x128>(__uint_as_float(lo ? b[0] : a[1]));
}
__device__ __forceinline__ void unpack8(u32x4 u, float* f) {
#pragma unroll
  for (int j = 0; j < 4; ++j) { f[2 * j] = lo16(u[j]); f[2 * j + 1] = hi16(u[j]); }
}
__device__ __forceinline__ void ln_row(const float (&v)[16], bool has2, float& mean, float& den) {
  float s = 0.f;
#pragma unroll
  for (int i = 0; i < 8; ++i) s += v[i];
  if (has2) {
#pragma unroll
    for (int i = 8; i < 16; ++i) s += v[i];
  }
  s = wsum(s);
  const float mu = s * (1.f / D);
  mean = rbf(mu);
  float q = 0.f;
#pragma unroll
  for (int i = 0; i < 8; ++i) { const float t = v[i] - mu; q = fmaf(t, t, q); }
  if (has2) {
#pragma unroll
    for (int i = 8; i < 16; ++i) { const float t = v[i] - mu; q = fmaf(t, t, q); }
  }
  q = wsum(q);
  const float var = rbf(q * (1.f / D));
  den = rbf(sqrtf(rbf(var + 1e-5f)));
}
__device__ __forceinline__ float pick2(u32 t, int e) { return (e & 1) ? hi16(t) : lo16(t); }
__device__ __forceinline__ void ln9(u32x4 x8, float v8, const float (&cb1)[9], const float (&sb)[9], u32x4& h8, u32& h1) {
  f32x2 v[4];
#pragma unroll
  for (int i = 0; i < 4; ++i) v[i] = up2(x8[i]);
  const f32x2 s2 = (v[0] + v[1]) + (v[2] + v[3]);
  const float mu = wsum((s2.x + s2.y) + v8) * (1.f / D);
  const f32x2 mu2 = {mu, mu};
  const float t8 = v8 - mu;
  f32x2 q = {t8 * t8, 0.f};
#pragma unroll
  for (int i = 0; i < 4; ++i) { const f32x2 t = v[i] - mu2; q = __builtin_elementwise_fma(t, t, q); }
  const float mean = rbf(mu);
  f32x2 d[4];
#pragma unroll
  for (int i = 0; i < 4; ++i) d[i] = rbf2(v[i] - f32x2{mean, mean});
  const float d8 = rbf(v8 - mean);
  const float var = rbf(wsum(q.x + q.y) * (1.f / D));
  const float rden = frcp(rbf(__builtin_amdgcn_sqrtf(rbf(var + 1e-5f))));
#pragma unroll
  for (int i = 0; i < 4; ++i) {
    const f32x2 t = rbf2(rbf2(d[i] * f32x2{rden, rden}) * f32x2{cb1[2 * i], cb1[2 * i + 1]}) + f32x2{sb[2 * i], sb[2 * i + 1]};
    h8[i] = cvt2(t.x, t.y);
  }
  h1 = cvt2(rbf(rbf(d8 * rden) * cb1[8]) + sb[8], 0.f);
}
template <int J0 = 0, int J1 = 9>
__device__ __forceinline__ void slab_dma(const u16* base, int stride_b, unsigned lds_dst, int wave, int lane) {
  const u32 v0 = (lane >> 3) * stride_b + (((lane & 7) ^ ((wave & 1) * 4 + (lane >> 4))) << 4), v1 = v0 ^ 32u;
  const u32 v4 = wave >= 4 ? v1 : v0;
  const char* gb = (const char*)base + (size_t)wave * 8 * stride_b;
#pragma unroll
  for (int j = J0; j < J1; ++j) dma(gb + (size_t)j * 64 * stride_b, j < 4 ? v0 : j == 4 ? v4 : v1, lds_dst + (j * 8 + wave) * 1024);
}
__device__ __forceinline__ void slab_frags(const unsigned char* lds, int base, int wave, int lane, u32x4 (&wf)[9]) {
  const int g = lane >> 4, q = (lane & 15) >> 2, p = lane & 3, hf = p >> 1;
  const int a0 = base + (hf * 288 + 8 * g + q) * 128 + ((wave ^ ((g & 1) * 4 + (q >> 1)) ^ (hf * 2)) << 4) + (p & 1) * 8, a1 = (a0 ^ 32) + 512;
#pragma unroll
  for (int s = 0; s < 9; ++s) {
    const u32x2 lo = trd(lds, a0 + s * 4096), hi = trd(lds, a1 + s * 4096);
    wf[s] = u32x4{lo[0], lo[1], hi[0], hi[1]};
  }
}
__device__ __forceinline__ void img_frags(const unsigned char* lds, int img, int lane, u32x4 (&af)[9]) {
#pragma unroll
  for (int s = 0; s < 9; ++s) af[s] = *(const u32x4*)(lds + img + (s * 64 + lane) * 16);
}
__device__ __forceinline__ f32x4 gemv9(const u32x4 (&wf)[9], const u32x4 (&af)[9]) {
  f32x4 acc[3];
  SCHED();
#pragma unroll
  for (int s = 0; s < 9; ++s) acc[s % 3] = mfma(wf[s], af[s], s < 3 ? f32x4{0.f, 0.f, 0.f, 0.f} : acc[s % 3]);
  return (acc[0] + acc[1]) + acc[2];
}
__device__ __forceinline__ int img_off(int b, int k8) {
  const int hf = k8 >= 36 ? 1 : 0, kk = k8 - hf * 36;
  return ((kk >> 2) * 64 + (kk & 3) * 16 + hf * 8 + b) * 16;
}
__device__ __forceinline__ u32 tag_x(u32x4 v, u32 e0, u32 e1) { return (v[2] ^ e0) | (v[3] ^ e1); }
__device__ __forceinline__ bool tag_ok(u32x4 v, u32 e0, u32 e1) { return tag_x(v, e0, e1) == 0u; }

constexpr int CODE_BYTES = 30592;

__global__ void __launch_bounds__(NT, 2) fused_kernel(const u16* __restrict__ x, const u16* __restrict__ kc, const u16* __restrict__ vc,
                                                      const u16* __restrict__ wq, const u16* __restrict__ wo, const u16* __restrict__ w_r,
                                                      const u16* __restrict__ wg, const u16* __restrict__ wu, const u16* __restrict__ wd,
                                                      const u16* __restrict__ c, const u16* __restrict__ w_mod, unsigned char* ws, u16* out) {
  __shared__ __attribute__((aligned(16))) unsigned char lds[LDS_BYTES];
  int w = blockIdx.x;
  const int tid = threadIdx.x, lane = tid & 63, wave = __builtin_amdgcn_readfirstlane(tid >> 6);
  const int n = lane & 15, grp = lane >> 4;
  const size_t pairbase = ((size_t)(w >> 1) * SK + (SK - WIN)) * 256 + (w & 1) * 16384;
  const int wpart = (wave & 3) * 4096 + (wave >> 2) * 262144;
  const int ko = wpart + (n >> 2) * 256 + grp * 64 + (n & 3) * 16;
  const char* kp = (const char*)kc + pairbase;
  u32x4 kb[32], vb[32];
#define KLD(u) do { unsigned long long t_; asm volatile("s_mov_b64 %1, %3\n\tglobal_load_dwordx4 %0, %2, %1 offset:%4 nt" : "=v"(kb[u]), "=&s"(t_) : "v"(ko), "s"(kp + ((u) >> 2) * 32768), "i"(((u) & 3) * 1024) : "memory"); } while (0)
  unsigned long long tst;
  asm volatile("s_memrealtime %0\n\tglobal_load_dwordx4 %1, %5, %6 nt\n\tglobal_load_dwordx4 %2, %5, %6 offset:1024 nt\n\t"
               "global_load_dwordx4 %3, %5, %6 offset:2048 nt\n\tglobal_load_dwordx4 %4, %5, %6 offset:3072 nt"
               : "=&s"(tst), "=&v"(kb[0]), "=&v"(kb[1]), "=&v"(kb[2]), "=&v"(kb[3]) : "v"(ko), "s"(kp) : "memory");
  asm volatile("s_waitcnt lgkmcnt(0)" : "+s"(w), "+s"(ws), "+s"(tst));
  const u32 tstart = (u32)tst;
  const int xcd = w & 7, idx = w >> 3;
  const rsrc_t rs = __builtin_amdgcn_make_buffer_rsrc(ws, 0, W_END, 0x00020000);
  const unsigned lds_base = (unsigned)(uintptr_t)(lds_t*)lds;
  const u32 tq0_ = tstart;
  u32 probe_ = 0;
  const unsigned long long epoch = (llvm_dispatch_id() + 1ull) * 0x9E3779B97F4A7C15ull;
  const u32 e0 = (u32)epoch, e1 = (u32)(epoch >> 32);
  unsigned long long pc0; asm volatile("s_getpc_b64 %0" : "=s"(pc0));
  asm volatile("" : "+s"(vc), "+s"(w_r), "+s"(wu), "+s"(wd), "+s"(out), "+s"(x), "+s"(w_mod), "+s"(wg), "+s"(wq), "+s"(wo));

  const int mw = idx < 6 ? idx * 8 + xcd : -1;
  const int md = mw < 18 ? mw : -1;
  const int me = mw >= 18 ? mw : -1;
  const int ml = mw >= 0 && mw < 6 ? 48 + mw : -1;
  const int qs = idx >= 6 && idx < 14 ? xcd * 8 + idx - 6 : -1;
  const int oj = idx >= 14 && idx < 23 ? idx - 14 : -1, op = xcd;
  const int gs = idx >= 23 ? xcd * 9 + idx - 23 : -1;

  const int s_att = w & 1, kvh = (w >> 1) & 15, b_att = w >> 5;
  {
    const char* vp = (const char*)vc + pairbase;
    const int vo = wpart + lane * 16;
#define VLD(u) do { unsigned long long t_; asm volatile("s_mov_b64 %1, %3\n\tglobal_load_dwordx4 %0, %2, %1 offset:%4 nt" : "=v"(vb[u]), "=&s"(t_) : "v"(vo), "s"(vp + ((u) >> 2) * 32768), "i"(((u) & 3) * 1024) : "memory"); } while (0)
#define TIE8(a, o) asm volatile("" : "+v"(a[o]), "+v"(a[(o) + 1]), "+v"(a[(o) + 2]), "+v"(a[(o) + 3]), "+v"(a[(o) + 4]), "+v"(a[(o) + 5]), "+v"(a[(o) + 6]), "+v"(a[(o) + 7]))
    const int pr_ = gs >= 0 ? PR2 : PR;
    const u32 tp0 = tstart + 9 * pr_ / 4 - TADJ;
#define PACE(i) do { _Pragma("nounroll") while ((int)((u32)__builtin_amdgcn_s_memrealtime() - tp0) < (i) * pr_ / 4) __builtin_amdgcn_s_sleep(2); SCHED(); } while (0)
#define REP4(M, u, W) do { M(u); W; M((u) + 1); W; M((u) + 2); W; M((u) + 3); W; } while (0)
    static_assert(DK <= 7 && DV <= 7, "the hand-placed waits assume it");
#define POISON() do { if (gs >= 0) { const int g_ = gs * PZ + tid; \
      if (tid < PZ && g_ < W_MS / 16) st128(rs, g_ * 16, u32x4{0xffffffffu, 0xffffffffu, 0xffffffffu, 0xffffffffu}); } SCHED(); } while (0)
#define PFLAG() do { if (gs >= 0 && tid == 448) st64(rs, W_FL + gs * 8, u32x2{e0, e1}); SCHED(); } while (0)
#define FOK(a) __all(((a[0] ^ e0) | (a[1] ^ e1) | tag_x(a, e0, e1)) == 0u)
#define FREQ() do { if (wave == 1) dma_sc(ws + W_FL, min(lane, NG / 2 - 1) * 16, lds_base + L_B + 2048); } while (0)
#define FCHK() do { if (wave == 1 && !FOK((*(const u32x4*)(lds + L_B + 2048 + lane * 16)))) { \
      _Pragma("nounroll") for (int sp = 0; sp < SPIN_CAP; ++sp) { nap(4); const u32x4 a_ = ld128(rs, W_FL + min(lane, NG / 2 - 1) * 16); if (FOK(a_)) break; } } SCHED(); } while (0)
#define SILU_IMAGE(c0, c1) do { float f[8]; unpack8(c0, f); u32x4 o; \
        _Pragma("unroll") for (int j = 0; j < 4; ++j) o[j] = cvt2(silu(f[2 * j]), silu(f[2 * j + 1])); \
        const int b_ = tid / 72, ch_ = tid - b_ * 72; \
        *(u32x4*)(lds + L_IMG + img_off(b_, ch_)) = o; \
        if (wave == 0) { unpack8(c1, f); \
          _Pragma("unroll") for (int j = 0; j < 4; ++j) o[j] = cvt2(silu(f[2 * j]), silu(f[2 * j + 1])); \
          *(u32x4*)(lds + L_IMG + img_off(7, 8 + lane)) = o; } } while (0)
    if (md >= 0) {
      u32x4 c0, c1, x0, x1; u32x2 xc;
      TSA(0);
      int ho = W_H + img_off(n & 7, (md >= 9 ? md - 9 : md) * 8 + wave) * 2 + (grp & 1) * 16;
      ald128(c0, c, tid * 16);
      ald128(c1, c, (512 + lane) * 16);
      ald128(x0, x, wave * 1152 + lane * 16);
      ald128(x1, x, wave * 1152 + 1024 + (lane & 7) * 16);
      ald64(xc, x, (n & 7) * 1152 + ((md >= 9 ? md - 9 : 0) * 64 + wave * 8 + (grp & 1) * 4) * 2);
      BAR();
      float* st = (float*)(lds + L_TILE + 1024);
      if (wave < 4) {
        slab_dma(w_mod + md * 64, NMOD * 2, lds_base + L_W, wave, lane);
        asm volatile("s_waitcnt vmcnt(9)" ::: "memory");
      } else asm volatile("s_waitcnt vmcnt(0)" ::: "memory");
      asm volatile("s_nop 0 ; both branches above cover V" : "+v"(c0), "+v"(c1), "+v"(x0), "+v"(x1), "+v"(xc) :: "memory");
      TSA(1);
      {
        SILU_IMAGE(c0, c1);
        float v[16]; unpack8(x0, v); unpack8(x1, v + 8);
        float mean, den; ln_row(v, lane < 8, mean, den);
        if (lane == 0) { st[wave] = mean; st[8 + wave] = den; }
      }
      TSA(2);
      if (wave >= 4) slab_dma(w_mod + md * 64, NMOD * 2, lds_base + L_W, wave, lane);
      TSA(3);
      lds_barrier();
      REP4(KLD, 4, );
      u32x4 wf[9], af[9];
      img_frags(lds, L_IMG, lane, af);
      const float mean = st[n & 7], rd = frcp(st[8 + (n & 7)]);
      float ln[4] = {rbf(rbf(lo16(xc[0]) - mean) * rd), rbf(rbf(hi16(xc[0]) - mean) * rd), rbf(rbf(lo16(xc[1]) - mean) * rd), rbf(rbf(hi16(xc[1]) - mean) * rd)};
#pragma unroll
      for (int s = 0; s < 9; ++s) asm volatile("" : "+v"(af[s]));
      asm volatile("" : "+v"(ln[0]), "+v"(ln[1]), "+v"(ln[2]), "+v"(ln[3]), "+v"(ho));
      WAITV(4); BAR(); SCHED();
      TSA(5);
      slab_frags(lds, L_W, wave, lane, wf);
      const f32x4 s = half_sum(gemv9(wf, af));
      STAMP(14); TSA(6);
      f32x4 r = s;
      if (md >= 9) {
#pragma unroll
        for (int j = 0; j < 4; ++j) r[j] = ln[j] * rbf(1.f + rbf(s[j]));
      }
      if (n < 8 && grp < 2) st128(rs, ho + (md >= 9 ? 18432 : 0), u32x4{cvt2(r[0], r[1]), cvt2(r[2], r[3]), e0, e1});
      SCHED();
      STAMP(15); TSA(7);
      BAR();
      KLD(8); KLD(9); WAITV(DK); KLD(10); WAITV(DK);
      KLD(11); WAITV(DK); REP4(KLD, 12, WAITV(DK)); REP4(KLD, 16, WAITV(DK));
      FREQ(); REP4(KLD, 20, WAITV(DK)); PACE(24); REP4(KLD, 24, WAITV(DK)); PACE(28); KLD(28); WAITV(DK); KLD(29);
      FCHK();
    } else {
      constexpr int L_LAND = 98304;
      const unsigned land = lds_base + L_LAND + wave * 6144;
      const unsigned char* lp = lds + L_LAND + wave * 6144 + lane * 16;
      const int sl_ = (3 - (int)((uintptr_t)wq >> 7)) & 7, sm_ = (63 - (int)((uintptr_t)wq >> 7)) & 63, sg_ = (3 - (int)((uintptr_t)wg >> 7)) & 7;
      const bool slowq = (qs >= 0 && ((qs & 7) == sl_ || qs == sm_)) || (gs >= 0 && (gs & 7) == sg_);
      if (oj >= 0 && wave < 2) {
        if (wave == 0 || (sm_ >> 3) == xcd) dma_keep((const char*)wq + (size_t)oj * 64 * 8192 + (wave == 0 ? xcd * 8 + sl_ : sm_) * 128, lane * 8192, land);
      } else if (gs >= 0 && wave < 2) {
        const int g0 = ((sg_ - xcd * 9) & 7) + wave * 8;
        if (g0 < 9) dma_keep((const char*)wg + (size_t)(idx - 23) * 64 * 9216 + (xcd * 9 + g0) * 128, lane * 9216, lds_base + L_B + 4096 + wave * 256);
      }
      POISON();
      if (oj >= 0 && oj < 8 && wave == 4)
        dma_keep((const void*)pc0, min((oj & 3) * 8192 + lane * 128, CODE_BYTES - 128), land);
      const u32 v0 = (lane >> 3) * 1152 + (((lane & 7) ^ ((lane >> 4) & 1)) << 4);
      const char* gb = (const char*)(wo + (size_t)(op * 512 + wave * 64) * D + oj * 64);
      if (me >= 0) { dma_sc(c, tid * 16, land); dma_sc(c, (512 + lane) * 16, land + 1024); }
      const bool fs = qs >= 0 || me >= 0;
      const u16* sbase = qs >= 0 ? wq + qs * 64 : gs >= 0 ? wg + gs * 64 : w_mod + me * 64;
      const int sstr = qs >= 0 ? NQ * 2 : gs >= 0 ? FF * 2 : NMOD * 2;
      const unsigned sdst = lds_base + L_W + (gs >= 0 ? 73728 : 0);
#define WK(j) do { if (oj >= 0) { if ((j) < 8) dma(gb + (j) * 8 * 1152, v0 ^ (((j) & 1) * 32 + ((j) >> 2) * 64), lds_base + L_W + (wave * 8 + (j)) * 1024); } \
                   else if (slowq) { if ((j) == SLOWJ) slab_dma(sbase, sstr, sdst, wave, lane); } \
                   else slab_dma<j, (j) + 1>(sbase, sstr, sdst, wave, lane); \
                   KLD((j) + 4); WAITV(DK); } while (0)
      WK(0); WK(1); WK(2); WK(3);
      WK(4); WK(5);
      static_assert(DK <= 5, "six younger requests cover the poison");
      if (me >= 0) { const u32x4 c0 = *(const u32x4*)lp, c1 = *(const u32x4*)(lp + 1024); SILU_IMAGE(c0, c1); }
      BAR();
      STAMP(2);
      PFLAG();
      WK(6); WK(7); WK(8);
      PACE(13); KLD(13); WAITV(DK); KLD(14); WAITV(DK); KLD(15); WAITV(DK);
      const int hg0 = (wave * 144 + lane) * 16, hg2 = (wave * 144 + 128 + n) * 16;
#define HREQ() do { dma_sc(ws + W_H, hg0, land); dma_sc(ws + W_H + 18432, hg0, land + 1024); dma_sc(ws + W_H + 1024, hg0, land + 2048); \
                    dma_sc(ws + W_H + 18432 + 1024, hg0, land + 3072); dma_sc(ws + W_H, hg2, land + 4096); dma_sc(ws + W_H + 18432, hg2, land + 5120); } while (0)
#define HGET() do { _Pragma("unroll") for (int i = 0; i < 6; ++i) hq[i] = *(const u32x4*)(lp + i * 1024); } while (0)
#define HQOK() __all((tag_x(hq[0], e0, e1) | tag_x(hq[1], e0, e1) | tag_x(hq[2], e0, e1) | tag_x(hq[3], e0, e1) | tag_x(hq[4], e0, e1) | tag_x(hq[5], e0, e1)) == 0u)
      PACE(16); KLD(16); WAITV(DK); KLD(17); WAITV(DK); PACE(18); KLD(18); WAITV(DK); KLD(19); WAITV(DK);
      TSA(4);
      if (qs >= 0) HREQ();
      PACE(20); FREQ(); KLD(20); WAITV(DK); KLD(21); WAITV(DK); PACE(22); KLD(22); WAITV(DK); KLD(23); WAITV(DK);
      if (qs >= 0) WAITV(4);
      TSA(5);
      PACE(24); KLD(24); KLD(25);
      if (qs >= 0) {
        u32x4 hq[6]; HGET();
        _Pragma("nounroll") for (int sp = 0; sp < SPIN_CAP && !HQOK(); ++sp) { nap(16); HREQ(); WAITV(0); HGET(); }
        STAMP(14); TSA(6);
#define HSUM(a, b) cvt2(lo16(a) + lo16(b), hi16(a) + hi16(b))
        *(u32x2*)(lds + L_IMG + hg0 / 2) = u32x2{HSUM(hq[0][0], hq[1][0]), HSUM(hq[0][1], hq[1][1])};
        *(u32x2*)(lds + L_IMG + 512 + hg0 / 2) = u32x2{HSUM(hq[2][0], hq[3][0]), HSUM(hq[2][1], hq[3][1])};
        if (lane < 16) *(u32x2*)(lds + L_IMG + hg2 / 2) = u32x2{HSUM(hq[4][0], hq[5][0]), HSUM(hq[4][1], hq[5][1])};
      } else WAITV(DK);
      KLD(26); KLD(27);
      if (fs) {
        u32x4 wf[9], af[9];
        lds_barrier();
        slab_frags(lds, L_W, wave, lane, wf);
        img_frags(lds, L_IMG, lane, af);
        const f32x4 s = half_sum(gemv9(wf, af));
        if (n < 8 && grp < 2) *(u32x2*)(lds + L_TILE + n * 128 + wave * 16 + grp * 8) = u32x2{cvt2(s[0], s[1]), cvt2(s[2], s[3])};
      } else WAITV(DK);
      PACE(28); KLD(28); KLD(29);
      WAITV(9); FCHK();
#define MPUB(m_) do { if (wave == 0) { const u32x4 t_ = *(const u32x4*)(lds + L_TILE + lane * 16); const int ch_ = ((m_) - 18) / 9, sl_2 = (m_) - 18 - ch_ * 9; \
          st128(rs, W_M + (ch_ * 8 + (lane >> 3)) * 1152 + sl_2 * 128 + (lane & 7) * 16, u32x4{sanu(t_[0]), sanu(t_[1]), sanu(t_[2]), sanu(t_[3])}); } } while (0)
      if (fs) {
        lds_barrier();
        if (qs < 0) MPUB(me);
        else if (wave < 2) {
          const u32x2 t2 = *(const u32x2*)(lds + L_TILE + tid * 8);
          st128(rs, W_Q + qs * 256 + (tid >> 4) * 16384 + n * 16, u32x4{t2[0], t2[1], e0, e1});
        }
        STAMP(15); TSA(7);
      }
      SCHED();
    }
    STAMP(3); TSB(0);
    const unsigned char* qg = ws + W_Q + (b_att * 1024 + kvh * 64) * 16;
    PACE(30); KLD(30); WAITV(DK); KLD(31); WAITV(DK);
    TSB(1);
    PACE(32); VLD(0); WAITV(DK); VLD(1); WAITV(DK); PACE(34); VLD(2); WAITV(DK); VLD(3); WAITV(DK);
    PACE(36); VLD(4); WAITV(DK); VLD(5); WAITV(DK);
    if (wave == 1) dma_sc(qg, lane * 16, lds_base + L_B + 1024);
    PACE(38); VLD(6); WAITV(DK); VLD(7); WAITV(DK);
    PACE(40); VLD(8); WAITV(DV); VLD(9); WAITV(DV);
    if (wave == 1) dma_sc(qg, lane * 16, lds_base + L_B + 1024);
    PACE(42); VLD(10); WAITV(DV); VLD(11); WAITV(DV);
    TIE8(kb, 0); TIE8(kb, 8); TIE8(kb, 16); TIE8(kb, 24);
    PACE(44); VLD(12); VLD(13);
    u32x4 qf[2];
    {
      const u32x4* ql = (const u32x4*)(lds + L_B + 1024 + (((n >> 2) & 1) * 32 + grp * 8 + (n & 3) * 2) * 16);
      u32x4 g[2];
      _Pragma("nounroll") for (int sp = 0; sp < SPIN_CAP; ++sp) {
        g[0] = ql[0]; g[1] = ql[1];
        if (__all((tag_x(g[0], e0, e1) | tag_x(g[1], e0, e1)) == 0u)) break;
        if (wave == 1) { dma_sc(qg, lane * 16, lds_base + L_B + 1024); WAITV(0); } else nap(4);
      }
      asm volatile("" ::: "memory");
      g[0] = ql[0]; g[1] = ql[1];
      qf[0] = n < 8 ? u32x4{g[0][0], g[0][1], g[1][0], g[1][1]} : u32x4{0, 0, 0, 0};
      qf[1] = n < 8 ? u32x4{0, 0, 0, 0} : u32x4{g[0][0], g[0][1], g[1][0], g[1][1]};
    }
    STAMP(4); TSB(2);
    constexpr float SCALE = 0.088388347648318440f;
    float sc[4];
#pragma unroll
    for (int i = 0; i < 4; ++i) {
      float y[2];
#pragma unroll
      for (int t = 0; t < 2; ++t) {
        const int u = 8 * t + 2 * i;
        const f32x4 a = mfma(kb[u + 16], qf[1], mfma(kb[u], qf[0], f32x4{0.f, 0.f, 0.f, 0.f}));
        const f32x4 b = mfma(kb[u + 17], qf[1], mfma(kb[u + 1], qf[0], f32x4{0.f, 0.f, 0.f, 0.f}));
        const float own = (n & 2) ? ((n & 1) ? b[3] : a[2]) : ((n & 1) ? b[1] : a[0]), oth = (n & 2) ? ((n & 1) ? a[3] : b[2]) : ((n & 1) ? a[1] : b[0]);
        y[t] = own + dppf<0xB1>(oth);
      }
      sc[i] = ((n & 2) ? y[1] : y[0]) + dppf<0x4E>((n & 2) ? y[0] : y[1]);
      if (i == 1 || i == 3) { SCHED(); if (i == 1) VLD(14); else VLD(15); SCHED(); }
    }
#pragma unroll
    for (int i = 0; i < 4; ++i) sc[i] = rbf(rbf(sc[i]) * SCALE);
    float mx = fmaxf(fmaxf(sc[0], sc[1]), fmaxf(sc[2], sc[3]));
    mx = fmaxf(mx, dppf<0xB1>(mx)); mx = fmaxf(mx, dppf<0x4E>(mx)); mx = fmaxf(mx, dppf<0x128>(mx));
    mx = xmax32(xmax16(mx));
#pragma unroll
    for (int i = 0; i < 4; ++i) sc[i] = __expf(sc[i] - mx);
    float ls = (sc[0] + sc[1]) + (sc[2] + sc[3]);
    ls += dppf<0xB1>(ls); ls += dppf<0x4E>(ls); ls += dppf<0x128>(ls);
    ls = xsum32(xsum16(ls));
    float* stat = (float*)(lds + L_B);
    if (tid == 0) *(u32*)(lds + L_B + 512) = 0u;
    if (lane == 0 || lane == 4) *(f32x2*)(stat + (wave * 2 + (lane >> 2)) * 2) = f32x2{mx, ls};
    SCHED(); PACE(48); VLD(16); VLD(17); SCHED();
    TSB(3);
    lds_barrier();
    TSB(4);
    float M = -INFINITY, L = 0.f;
    {
      f32x2 t[8];
#pragma unroll
      for (int q = 0; q < 8; ++q) { t[q] = *(const f32x2*)(stat + (q * 2 + ((n >> 2) & 1)) * 2); M = fmaxf(M, t[q].x); }
#pragma unroll
      for (int q = 0; q < 8; ++q) L = fmaf(__expf(t[q].x - M), t[q].y, L);
    }
    if (tid < 8) st128(rs, W_ST + (w * 8 + lane) * 16, u32x4{__float_as_uint(M), __float_as_uint(L), e0, e1});
    SCHED();
#define VTR(G) do { u32x4 f_[8]; _Pragma("unroll") for (int t = 0; t < 8; ++t) _Pragma("unroll") for (int j = 0; j < 4; ++j) \
      f_[t][j] = (t & 1) ? phi(vb[(G) * 8 + 2 * j][t >> 1], vb[(G) * 8 + 2 * j + 1][t >> 1]) : plo(vb[(G) * 8 + 2 * j][t >> 1], vb[(G) * 8 + 2 * j + 1][t >> 1]); \
      _Pragma("unroll") for (int t = 0; t < 8; ++t) { vb[(G) * 8 + t] = f_[t]; asm volatile("" : "+v"(vb[(G) * 8 + t])); } SCHED(); } while (0)
    WAITV(DV);
    TIE8(vb, 0);
    VTR(0);
    if (gs >= 0) {
      const unsigned char* mb = ws + W_M + (8 + wave) * 1152;
      const unsigned ld_ = lds_base + L_W + wave * 2304;
      dma_sc(mb, lane * 16, ld_); dma_sc(mb + 9216, lane * 16, ld_ + 1024);
      if (lane < 16) dma_sc(mb + 1024, (lane >> 3) * 9216 + (lane & 7) * 16, ld_ + 2048);
    }
    PACE(50); VLD(18); WAITV(DV); VLD(19); WAITV(DV);
    PACE(52); VLD(20); WAITV(5);
    TIE8(vb, 8);
    VTR(1);
    VLD(21); WAITV(DV);
    const int po = W_ST + ((w ^ 1) * 8 + (n & 4)) * 16;
    PACE(54);
    VLD(22); WAITV(DV); VLD(23); WAITV(DV);
    u32x4 ps;
    ald128sc(ps, ws, po);
    PACE(56);
    VLD(24); WAITV(DV); VLD(25); WAITV(DV);
    PACE(58);
    VLD(26); WAITV(DV); VLD(27); WAITV(DV);
    VLD(28); VLD(29); WAITV(6);
    asm volatile("" : "+v"(ps));
    TIE8(vb, 16);
    VLD(30); VLD(31);
    VTR(2);
    if (!__all(tag_ok(ps, e0, e1))) {
      int sp = 0;
      _Pragma("nounroll") do { nap(1); ps = ld128(rs, po); } while (!__all(tag_ok(ps, e0, e1)) && ++sp < SPIN_CAP);
    }
    float fac;
    {
      const float M2 = __uint_as_float(ps[0]), L2 = __uint_as_float(ps[1]);
      const float Mg = fmaxf(M, M2);
      const float Lg = __expf(M - Mg) * L + __expf(M2 - Mg) * L2;
      fac = __expf(mx - Mg) / Lg;
    }
    STAMP(1); TSB(5);
    u32x4 pf[4];
#pragma unroll
    for (int j = 0; j < 4; ++j) {
      const float p_ = sc[j] * fac;
      pf[0][j] = cvt2(p_, dppf<0xB1>(p_));
      pf[1][j] = dppu<0x102>(pf[0][j]); pf[2][j] = dppu<0x108>(pf[0][j]); pf[3][j] = dppu<0x10A>(pf[0][j]);
    }
    f32x4 oa[8];
#pragma unroll
    for (int t = 0; t < 8; ++t) oa[t] = f32x4{0.f, 0.f, 0.f, 0.f};
    SCHED();
#pragma unroll
    for (int G = 0; G < 2; ++G)
#pragma unroll
      for (int t = 0; t < 8; ++t) oa[t] = mfma(pf[G], vb[G * 8 + t], oa[t]);
    SCHED();
#pragma unroll
    for (int t = 0; t < 8; ++t) oa[t] = mfma(pf[2], vb[16 + t], oa[t]);
    SCHED();
    int t2_ = threadIdx.x; asm volatile("" : "+v"(t2_));
    const int tid = t2_, lane = tid & 63, n = lane & 15, grp = lane >> 4;
    int oo = W_O + ((kvh * 8 + b_att) * 2 + s_att) * 1024 + lane * 16;
    int mw_ = L_C + wave * 1024 + lane * 32, mr_ = L_C + (grp * 16 + (lane & 7) * 2 + (n >> 3)) * 16;
    asm volatile("" : "+v"(oo), "+v"(mw_), "+v"(mr_), "+v"(pf[3]));
    const bool late = gs >= 0;
    u32x4 bg[9], mg[2]; u32 mh[2];
    const u32 dv = (lane >> 3) * 128 + (((lane & 7) ^ ((lane >> 4) & 3)) << 4);
    const char* db = (const char*)(wd + (size_t)gs * 64 * D) + wave * 1024;
    if (late) {
      slab_frags(lds, L_W + 73728, wave, lane, bg);
#pragma unroll
      for (int i = 0; i < 2; ++i) { mg[i] = *(const u32x4*)(lds + L_W + wave * 2304 + i * 1024 + lane * 16); mh[i] = *(const u32*)(lds + L_W + wave * 2304 + 2048 + i * 128 + (lane >> 1) * 4); }
      lds_barrier();
#pragma unroll
      for (int j = 0; j < 4; ++j) dma(db + j * 8192, dv, lds_base + L_W + 73728 + (j * 8 + wave) * 1024);
      WAITV(6);
    } else {
      if (oj >= 0) {
        const int q = n >> 2, p = lane & 3, hf = p >> 1;
        const int a0 = L_W + (hf * 32 + 8 * grp + q) * 128 + (((q >> 1) ^ ((grp & 1) * 2) ^ (hf * 4) ^ wave) << 4) + (p & 1) * 8;
#pragma unroll
        for (int k = 0; k < 8; ++k) {
          const u32x2 lo = trd(lds, a0 + k * 8192), hi = trd(lds, a0 + k * 8192 + 512);
          bg[k] = u32x4{lo[0], lo[1], hi[0], hi[1]};
        }
#pragma unroll
        for (int k = 0; k < 8; ++k) asm volatile("" : "+v"(bg[k]));
      }
      WAITV(2);
    }
    u32x4 vf[8];
    asm volatile("s_nop 0 ; both branches above cover V up to the last two" : "+v"(vb[24]), "+v"(vb[25]), "+v"(vb[26]), "+v"(vb[27]), "+v"(vb[28]), "+v"(vb[29]));
#pragma unroll
    for (int t = 0; t < 8; ++t)
#pragma unroll
      for (int j = 0; j < 3; ++j) vf[t][j] = (t & 1) ? phi(vb[24 + 2 * j][t >> 1], vb[25 + 2 * j][t >> 1]) : plo(vb[24 + 2 * j][t >> 1], vb[25 + 2 * j][t >> 1]);
#pragma unroll
    for (int t = 0; t < 8; ++t) asm volatile("" : "+v"(vf[t][0]), "+v"(vf[t][1]), "+v"(vf[t][2]));
    if (late) WAITV(4); else WAITV(0);
    asm volatile("s_nop 0 ; both branches above cover V" : "+v"(vb[30]), "+v"(vb[31]));
#pragma unroll
    for (int t = 0; t < 8; ++t) {
      vf[t][3] = (t & 1) ? phi(vb[30][t >> 1], vb[31][t >> 1]) : plo(vb[30][t >> 1], vb[31][t >> 1]);
      oa[t] = mfma(pf[3], vf[t], oa[t]);
    }
    STAMP(5); TSB(6);
    if (grp < 2) {
      *(f32x4*)(lds + mw_) = f32x4{oa[0][0], oa[1][0], oa[2][0], oa[3][0]};
      *(f32x4*)(lds + mw_ + 16) = f32x4{oa[4][0], oa[5][0], oa[6][0], oa[7][0]};
    }
    lds_barrier();
    if (wave == 0) {
      f32x4 m[8];
#pragma unroll
      for (int q = 0; q < 8; ++q) m[q] = *(const f32x4*)(lds + mr_ + q * 1024);
      SCHED();
      const f32x4 o = ((m[0] + m[1]) + (m[2] + m[3])) + ((m[4] + m[5]) + (m[6] + m[7]));
      st128(rs, oo, u32x4{sanf(o[0]), sanf(o[1]), sanf(o[2]), sanf(o[3])});
    }
    STAMP(6); TSB(7); SCHED();
  if (oj >= 0) {
    int iw = L_OI + wave * 1024 + ((lane & 3) * 16 + ((lane >> 2) & 1) * 8 + ((lane >> 3) ^ (lane & 7))) * 16, ir = L_OI + (lane ^ ((n >> 3) * 4 + grp)) * 16;
    int rw = L_OI + 8192 + (n & 7) * 272 + wave * 32 + (grp & 1) * 16 + (grp >> 1) * 8 + (n >> 3) * 4, rr = L_OI + 8192 + wave * 272 + ((n & 7) * 2 + (n >> 3)) * 16;
    int xo = W_XP + (wave * 9 + oj) * 2048 + (n >> 3) * 1024 + op * 128 + (n & 7) * 16;
    int ob = W_O + ((op * 2 + (wave >> 2)) * 8 + (lane >> 3)) * 2048 + (wave & 3) * 256 + (lane & 7) * 16;
    asm volatile("" : "+v"(rw), "+v"(rr), "+v"(xo), "+v"(ob), "+v"(iw), "+v"(ir));
    TSC(0);
    nap(4);
    u32x4 a[4];
    _Pragma("nounroll") for (int sp = 0; sp < SPIN_CAP; ++sp) {
#pragma unroll
      for (int p = 0; p < 4; ++p) a[p] = ld128(rs, ob + (p >> 1) * 1024 + (p & 1) * 128);
      if (__all(mxu(mxu(mx4(a[0]), mx4(a[1])), mxu(mx4(a[2]), mx4(a[3]))) != 0xffffffffu)) break;
      nap(2);
    }
    STAMP(7); TSC(1);
#define OS(p, j) (__uint_as_float(a[p][j]) + __uint_as_float(a[(p) + 2][j]))
    *(u32x4*)(lds + iw) = u32x4{cvt2(OS(0, 0), OS(0, 1)), cvt2(OS(0, 2), OS(0, 3)), cvt2(OS(1, 0), OS(1, 1)), cvt2(OS(1, 2), OS(1, 3))};
    lds_barrier();
    TSC(2);
    u32x4 of[8];
#pragma unroll
    for (int k = 0; k < 8; ++k) { of[k] = *(const u32x4*)(lds + ir + k * 1024); SCHED(); }
    f32x4 acc = {0.f, 0.f, 0.f, 0.f};
#pragma unroll
    for (int k = 0; k < 8; ++k) acc = mfma(bg[k], of[k], acc);
    *(u32*)(lds + rw) = sanf(half_sum1(acc, n < 8));
    lds_barrier();
    TSC(3);
    if (lane < 16) st128(rs, xo, *(const u32x4*)(lds + rr));
    TSC(4);
  }
  if (oj >= 0) STAMP(13);
  if (gs >= 0) {
    constexpr int L_H2 = L_B;
    constexpr int L_ACT = L_C + 4608;
    const int b = wave;
    u32x4 bu[9], wdf[2][5];
    BAR(); SCHED();
    TSC(0);
    float sb[9], cb1[9];
    int ia[2], ra[4], yo[3], tw, tr_, xa, xb;
    u32x4 x8, fy = {e0, e1, e0, e1}; u32 x1;
    int fo = W_FY + min(lane, NPY / 2 - 1) * 16;
    {
#pragma unroll
      for (int j = 4; j < 9; ++j) dma(db + j * 8192, dv, lds_base + L_W + 73728 + (j * 8 + wave) * 1024);
      BAR();
      slab_dma(wu + gs * 64, FF * 2, lds_base + L_W, wave, lane);
#pragma unroll
    for (int i = 0; i < 2; ++i) {
      const int k = i == 0 ? lane * 8 : 512 + lane, hf = k >= 288 ? 1 : 0, kk = k - hf * 288, s = kk >> 5, g = (kk >> 3) & 3;
      ia[i] = L_H2 + (s * 64 + g * 16 + ((hf * 8 + b) ^ ((s & 3) * 4 + g))) * 16 + (kk & 7) * 2;
    }
#pragma unroll
    for (int i = 0; i < 4; ++i) ra[i] = L_H2 + (grp * 16 + (n ^ (i * 4 + grp))) * 16;
    tw = L_ACT + (n & 7) * 144 + (wave >> 1) * 32 + (grp & 1) * 16 + (wave & 1) * 8 + (grp >> 1) * 4 + (n >> 3) * 2; tr_ = L_ACT + ((n >> 2) * 2 + (n & 1)) * 144 + grp * 16;
    yo[0] = W_Y + (((grp * 2) * 18 + wave * 2 + (n >> 3)) * NG + gs) * 128 + (n & 7) * 16;
    yo[1] = W_Y + ((grp * 2 * 18 + 16 + ((wave >> 1) & 1)) * NG + gs) * 128 + (wave & 1) * 64 + n * 4;
    yo[2] = yo[0] + 18 * NG * 128;
    xa = W_X1 + (xcd * 8 + b) * 1152 + lane * 16; xb = W_X1 + (xcd * 8 + b) * 1152 + 1024 + (lane >> 1) * 4;
    asm volatile("" : "+v"(ia[0]), "+v"(ia[1]), "+v"(ra[0]), "+v"(ra[1]), "+v"(ra[2]), "+v"(ra[3]), "+v"(tw), "+v"(tr_), "+v"(yo[0]), "+v"(yo[1]), "+v"(yo[2]), "+v"(xa), "+v"(xb), "+v"(fo));
#define MOK() __all(mxu(mxu(mx4(mg[0]), mx4(mg[1])), mxu(mh[0], mh[1])) != 0xffffffffu)
#define MLD(b_) do { _Pragma("unroll") for (int i = 0; i < 2; ++i) { mg[i] = ld128(rs, W_M + (8 + i * 8 + (b_)) * 1152 + lane * 16); mh[i] = ld32(rs, W_M + (8 + i * 8 + (b_)) * 1152 + 1024 + (lane >> 1) * 4); } } while (0)
#define MUNP() do { _Pragma("unroll") for (int i = 0; i < 4; ++i) { sb[2 * i] = lo16(mg[0][i]); sb[2 * i + 1] = hi16(mg[0][i]); \
                      cb1[2 * i] = rbf(1.f + lo16(mg[1][i])); cb1[2 * i + 1] = rbf(1.f + hi16(mg[1][i])); } \
                    sb[8] = pick2(mh[0], lane); cb1[8] = rbf(1.f + pick2(mh[1], lane)); \
                    _Pragma("unroll") for (int i = 0; i < 9; ++i) asm volatile("" : "+v"(sb[i]), "+v"(cb1[i])); } while (0)
      if (!MOK()) {
        int sp = 0;
        _Pragma("nounroll") do { nap(2); MLD(b); } while (!MOK() && ++sp < SPIN_CAP);
      }
      MUNP();
#pragma unroll
      for (int s = 0; s < 9; ++s) asm volatile("" : "+v"(bg[s]));
      asm volatile("s_waitcnt vmcnt(9)\n\ts_barrier" ::: "memory");
      {
        const int q = n >> 2, p = lane & 3, r = 16 * (grp >> 1) + 4 * (grp & 1) + q;
        const int a0 = L_W + 73728 + r * 1152 + (p & 1) * 8;
#pragma unroll
        for (int t = 0; t < 5; ++t) {
          const int a = t < 4 ? a0 + wave * 128 + (((t + (p >> 1) * 4) ^ (((r + wave) >> 1) & 3)) << 4) : a0 + 1024 + ((((wave & 3) * 2 + (p >> 1)) ^ ((r >> 1) & 3)) << 4);
#pragma unroll
          for (int ks = 0; ks < 2; ++ks) {
            const u32x2 lo = trd(lds, a + ks * 36864), hi = trd(lds, a + ks * 36864 + 9216);
            wdf[ks][t] = u32x4{lo[0], lo[1], hi[0], hi[1]};
          }
        }
#pragma unroll
        for (int t = 0; t < 5; ++t) asm volatile("" : "+v"(wdf[0][t]), "+v"(wdf[1][t]));
      }
      TSC(1);
      asm volatile("s_waitcnt vmcnt(0)\n\ts_barrier" ::: "memory");
      x8 = ld128(rs, xa); x1 = ld32(rs, xb);
      slab_frags(lds, L_W, wave, lane, bu);
      STAMP(13);
#pragma unroll
      for (int s = 0; s < 9; ++s) asm volatile("" : "+v"(bu[s]));
    }
    TSC(2);
    {
      TSC(3);
#define XOK() __all(mxu(mx4(x8), x1) != 0xffffffffu)
      if (!XOK()) {
        int sp = 0;
        _Pragma("nounroll") do { nap(1); x8 = ld128(rs, xa); x1 = ld32(rs, xb); } while (!XOK() && ++sp < SPIN_CAP);
      }
      STAMP(9); TSC(4);
      if (wave == 0) fy = ld128(rs, fo);
      SCHED();
      u32x4 h8; u32 h1;
      ln9(x8, pick2(x1, lane), cb1, sb, h8, h1);
      *(u32x4*)(lds + ia[0]) = h8;
      *(u16*)(lds + ia[1]) = (u16)h1;
      TSC(5);
      lds_barrier();
      u32x4 af[9];
#pragma unroll
      for (int s = 0; s < 9; ++s) { af[s] = *(const u32x4*)(lds + ra[s & 3] + s * 1024); SCHED(); }
      f32x4 ag = {0.f, 0.f, 0.f, 0.f}, au = {0.f, 0.f, 0.f, 0.f};
#pragma unroll
      for (int s = 0; s < 9; ++s) ag = mfma(bg[s], af[s], ag);
      SCHED();
#pragma unroll
      for (int s = 0; s < 9; ++s) au = mfma(bu[s], af[s], au);
      const float g = rbf(silu(rbf(half_sum1(ag, n < 8))));
#pragma unroll
      for (int s = 0; s < 9; ++s) { __builtin_amdgcn_sched_group_barrier(0x8, 1, 0); __builtin_amdgcn_sched_group_barrier(0x2, 3, 0); }
      SCHED();
      *(u16*)(lds + tw) = (u16)cvt2(g * rbf(half_sum1(au, n < 8)), 0.f);
      SCHED();
      if (wave == 0 && !FOK(fy)) {
        int sp = 0;
        _Pragma("nounroll") do { nap(2); fy = ld128(rs, fo); } while (!FOK(fy) && ++sp < SPIN_CAP);
      }
    }
    lds_barrier();
    STAMP(10); TSC(6);
    {
      u32x4 af[2];
#pragma unroll
      for (int ks = 0; ks < 2; ++ks) af[ks] = *(const u32x4*)(lds + tr_ + ks * 64);
      f32x4 d[5];
#pragma unroll
      for (int t = 0; t < 4; ++t) d[t] = mfma(af[1], wdf[1][t], mfma(af[0], wdf[0][t], f32x4{0.f, 0.f, 0.f, 0.f}));
#pragma unroll
      for (int j = 0; j < 2; ++j)
        st128(rs, yo[2 * j], u32x4{sanf(d[0][j]), sanf(d[1][j]), sanf(d[2][j]), sanf(d[3][j])});
      SCHED();
      if (wave < 4) {
        d[4] = mfma(af[1], wdf[1][4], mfma(af[0], wdf[0][4], f32x4{0.f, 0.f, 0.f, 0.f}));
        __builtin_amdgcn_raw_buffer_store_b32(sanf(d[4][0]), rs, yo[1], 0, 16);
        __builtin_amdgcn_raw_buffer_store_b32(sanf(d[4][1]), rs, yo[1], 18 * NG * 128, 16);
      }
      TSC(7);
    }
  }
  {
  int t3_ = threadIdx.x; asm volatile("" : "+v"(t3_));
  const int tid = t3_, lane = tid & 63, n = lane & 15, grp = lane >> 4;
  const u32 t6_ = (u32)__builtin_amdgcn_s_memrealtime();
  if (idx < 12 && wave >= 6) {
    const int g0 = ((3 - (int)((uintptr_t)wu >> 7) - xcd * 9) & 7) + (wave - 6) * 8;
    UNTIL(t6_ + 15);
    if (g0 < 9) dma_keep(wu, (idx * 48 + min(lane, 47)) * 9216 + (xcd * 9 + g0) * 128, lds_base + L_B + 4096 + wave * 256);
  }
  if ((qs >= 0 || mw >= 6) && wave >= 2) {
    const int py = qs >= 0 ? qs : 58 + mw;
    UNTIL(t6_ + 20);
#pragma unroll
    for (int i = 0; i < 3; ++i) {
      const int k_ = i * 384 + tid - 128, g_ = py * PY + k_;
      if (k_ < PY && g_ < 144 * NG * 8) st128(rs, W_Y + g_ * 16, u32x4{0xffffffffu, 0xffffffffu, 0xffffffffu, 0xffffffffu});
    }
    WAITV(0);
    if (lane == 0 && __hip_atomic_fetch_add((__attribute__((address_space(3))) u32*)((lds_t*)lds + L_B + 512), 1u, __ATOMIC_RELAXED, __HIP_MEMORY_SCOPE_WORKGROUP) == 5u)
      st64(rs, W_FY + py * 8, u32x2{e0, e1});
  }
  if (ml >= 0) {
    BAR();
    slab_dma(w_mod + ml * 64, NMOD * 2, lds_base + L_W, wave, lane);
    u32x4 wf[9], af[9];
    img_frags(lds, L_IMG, lane, af);
    asm volatile("s_waitcnt vmcnt(0)\n\ts_barrier" ::: "memory");
    slab_frags(lds, L_W, wave, lane, wf);
    const f32x4 s = half_sum(gemv9(wf, af));
    if (n < 8 && grp < 2) *(u32x2*)(lds + L_TILE + n * 128 + wave * 16 + grp * 8) = u32x2{cvt2(s[0], s[1]), cvt2(s[2], s[3])};
    lds_barrier();
    MPUB(ml);
  }

  if (idx >= 5 && idx < 14 && wave == 0) {
    const int b = xcd, sl = idx - 5, g = lane & 7;
    const rsrc_t rx = __builtin_amdgcn_make_buffer_rsrc((void*)x, 0, 8 * D * 2, 0x00020000);
    float xv[2], ga[2];
#pragma unroll
    for (int i = 0; i < 2; ++i) xv[i] = b2f(__builtin_amdgcn_raw_buffer_load_b16(rx, (b * D + sl * 64 + g * 8 + i * 4 + grp) * 2, 0, 0));
    u32 mm[2];
    _Pragma("nounroll") for (int sp = 0; sp < SPIN_CAP; ++sp) {
#pragma unroll
      for (int i = 0; i < 2; ++i) mm[i] = ld32(rs, W_M + b * 1152 + sl * 128 + g * 16 + i * 8 + (grp >> 1) * 4);
      if (__all(mxu(mm[0], mm[1]) != 0xffffffffu)) break;
      nap(4);
    }
    ga[0] = pick2(mm[0], grp); ga[1] = pick2(mm[1], grp);
    int xi = W_XP + (b * 9 + sl) * 2048 + lane * 16, xo = W_X1 + ((lane >> 3) * 8 + b) * 1152 + sl * 128 + g * 16;
    asm volatile("" : "+v"(xv[0]), "+v"(xv[1]), "+v"(ga[0]), "+v"(ga[1]), "+v"(xi), "+v"(xo));
    TSC(0);
    u32x4 xp[2];
    _Pragma("nounroll") for (int sp = 0; sp < SPIN_CAP; ++sp) {
      xp[0] = ld128(rs, xi); xp[1] = ld128(rs, xi + 1024);
      if (__all(mxu(mx4(xp[0]), mx4(xp[1])) != 0xffffffffu)) break;
      nap(1);
    }
    TSC(1);
    const u32x2 r0 = gather4(rbf(xv[0] + rbf(ga[0] * rbf(red8(asf4(xp[0])))))), r1 = gather4(rbf(xv[1] + rbf(ga[1] * rbf(red8(asf4(xp[1]))))));
    st128(rs, xo, u32x4{sanu(r0[0]), sanu(r0[1]), sanu(r1[0]), sanu(r1[1])});
    if (lane == 0) st128(rs, W_TS + (sl * 8 + b) * 128, u32x4{(u32)__builtin_amdgcn_s_memrealtime(), 0u, e0, e1});
    STAMP(8); TSC(2);
  }

  if (idx >= 5 && idx < 23 && wave == 1) {
    const int b = xcd, jh = idx - 5, col = jh * 32 + (jh < 16 ? (lane & 7) + grp * 8 : (lane & 7) * 4 + grp);
    const rsrc_t rw_ = __builtin_amdgcn_make_buffer_rsrc((void*)w_r, 0, D * 2, 0x00020000);
    const u32x4 wr8 = __builtin_amdgcn_raw_buffer_load_b128(rw_, lane * 16, 0, 0);
    const float wr1 = b2f(__builtin_amdgcn_raw_buffer_load_b16(rw_, 1024 + lane * 2, 0, 0));
    u32x4 mg[2]; u32 mh[2];
    _Pragma("nounroll") for (int sp = 0; sp < SPIN_CAP; ++sp) { MLD(b); if (MOK()) break; nap(4); }
    float sb[9], cb1[9];
    MUNP();
    const int xa = W_X1 + (xcd * 8 + b) * 1152 + lane * 16, xb = W_X1 + (xcd * 8 + b) * 1152 + 1024 + (lane >> 1) * 4;
    u32x4 x8; u32 x1;
    _Pragma("nounroll") for (int sp = 0; sp < SPIN_CAP; ++sp) { x8 = ld128(rs, xa); x1 = ld32(rs, xb); if (XOK()) break; nap(1); }
    const u32 tx_ = (u32)__builtin_amdgcn_s_memrealtime();
    const int go = W_M + (24 + b) * 1152 + (col >> 1) * 4, fo = W_FY + min(lane, NPY / 2 - 1) * 16;
    u32 gq = ld32(rs, go); u32x4 fy = ld128(rs, fo);
    const float v8 = pick2(x1, lane);
    u32x4 h8; u32 h1;
    ln9(x8, v8, cb1, sb, h8, h1);
    unsigned char* row = lds + L_C;
    *(u32x4*)(row + lane * 16) = x8; *(u32x4*)(row + 1152 + lane * 16) = h8;
    *(u16*)(row + 1024 + lane * 2) = (u16)(__float_as_uint(v8) >> 16); *(u16*)(row + 2176 + lane * 2) = (u16)h1;
    float r = lo16(h1) * wr1;
#pragma unroll
    for (int i = 0; i < 4; ++i) r = fmaf(hi16(h8[i]), hi16(wr8[i]), fmaf(lo16(h8[i]), lo16(wr8[i]), r));
    r = rbf(wsum(r));
    float rsel = rbf(1.f / (1.f + __expf(-r)));
    float x1v = b2f(*(const u16*)(row + col * 2)), h2 = b2f(*(const u16*)(row + 1152 + col * 2));
    _Pragma("nounroll") for (int sp = 0; sp < SPIN_CAP && !(__all(gq != 0xffffffffu) && FOK(fy)); ++sp) { nap(2); gq = ld32(rs, go); fy = ld128(rs, fo); }
    float gbv = pick2(gq, col);
    u32 tgo;
    {
      const u32x4 tq = ld128(rs, W_TS + lane * 128);
      int d_ = tag_ok(tq, e0, e1) ? (int)(tq[0] - tx_) : 0;
      d_ = max(d_, (int)dppu<0xB1>(d_)); d_ = max(d_, (int)dppu<0x4E>(d_)); d_ = max(d_, (int)dppu<0x141>(d_)); d_ = max(d_, (int)dppu<0x140>(d_));
      d_ = max(max(__builtin_amdgcn_readlane(d_, 0), __builtin_amdgcn_readlane(d_, 16)), max(__builtin_amdgcn_readlane(d_, 32), __builtin_amdgcn_readlane(d_, 48)));
      tgo = tx_ + min(d_ + TGO, TGO - 15);
    }
    int yi = W_Y + (b * 18 + jh) * NG * 128 + lane * 16, oo = (b * D + col) * 2;
    asm volatile("" : "+v"(rsel), "+v"(x1v), "+v"(h2), "+v"(gbv), "+v"(yi), "+v"(oo));
    TSC(5);
    UNTIL(tgo);
    float z;
    _Pragma("nounroll") for (int sp = 0; sp < SPIN_CAP; ++sp) {
      u32x4 y[9];
#pragma unroll
      for (int i = 0; i < 9; ++i) y[i] = ld128(rs, yi + i * 1024);
      z = red8((((asf4(y[0]) + asf4(y[1])) + (asf4(y[2]) + asf4(y[3]))) + ((asf4(y[4]) + asf4(y[5])) + (asf4(y[6]) + asf4(y[7])))) + asf4(y[8]));
      if (__all(z == z)) break;
      u32 m = mx4(y[8]);
#pragma unroll
      for (int i = 0; i < 8; ++i) m = mxu(m, mx4(y[i]));
      if (__all(m != 0xffffffffu)) break;
      nap(1);
    }
    STAMPW(11); TSC(6);
    const float y2 = rbf(rbf(z) * rsel);
    const float res = x1v + rbf(gbv * rbf(h2 + y2));
    if (!(lane & 8)) __builtin_amdgcn_raw_buffer_store_b16((u16)(cvt2(res, 0.f) & 0xffffu), __builtin_amdgcn_make_buffer_rsrc(out, 0, 8 * D * 2, 0x00020000), oo, 0, 0);
    STAMPW(12); TSC(7);
  }
  TSOUT();
  _Pragma("nounroll") while ((u32)__builtin_amdgcn_s_memrealtime() - tq0_ < 4000u + probe_) __builtin_amdgcn_s_sleep(2);
  }
  }
}

extern "C" void kernel_launch(void* const* d_in, const int* in_sizes, int n_in, void* d_out, int out_size, void* d_ws, size_t ws_size,
                              hipStream_t stream) {
  fused_kernel<<<NWG, NT, 0, stream>>>((const u16*)d_in[0], (const u16*)d_in[1], (const u16*)d_in[2], (const u16*)d_in[3], (const u16*)d_in[4],
                                       (const u16*)d_in[6], (const u16*)d_in[7], (const u16*)d_in[8], (const u16*)d_in[9], (const u16*)d_in[10],
                                       (const u16*)d_in[11], (unsigned char*)d_ws, (u16*)d_out);
}
```

```cpp
#include <hip/hip_runtime.h>
#include <hip/hip_bf16.h>
#include <cstdint>

typedef unsigned u32;
typedef unsigned short u16;
typedef u32 u32x4 __attribute__((ext_vector_type(4)));
typedef u32 u32x2 __attribute__((ext_vector_type(2)));
typedef float f32x4 __attribute__((ext_vector_type(4)));
typedef float f32x2 __attribute__((ext_vector_type(2)));
typedef short s16x4 __attribute__((ext_vector_type(4)));
typedef __bf16 bf16x8 __attribute__((ext_vector_type(8)));
typedef __bf16 bf16x2 __attribute__((ext_vector_type(2)));
typedef __attribute__((address_space(3))) unsigned char lds_t;

constexpr int D = 576, SK = 8192, WIN = 2048, FF = 4608, NQ = 4096, NMOD = 6 * D;
constexpr int NWG = 256, NT = 512, NG = 72;
constexpr int SPIN_CAP = 1 << 15;

constexpr int W_O = 0;
constexpr int W_XP = W_O + 262144;
constexpr int W_X1 = W_XP + 147456;
constexpr int W_M = W_X1 + 73728;
constexpr int W_MS = W_M + 36864;
constexpr int W_Y = W_MS;
constexpr int W_FL = W_Y + 144 * NG * 128;
constexpr int W_FY = W_FL + 1024;
constexpr int W_H = W_FY + 1024;
constexpr int W_Q = W_H + 36864;
constexpr int W_ST = W_Q + 131072;
constexpr int W_TS = W_ST + 32768;
constexpr int W_END = W_TS + 72 * 128;
constexpr int PZ = (W_MS / 16 + NG - 1) / NG;
constexpr int NPY = 106, PY = (144 * NG * 8 + NPY - 1) / NPY;
static_assert(PZ <= NT && PY <= 3 * 384, "poison split");

constexpr int L_W = 0;
constexpr int L_IMG = 73728;
constexpr int L_TILE = L_IMG + 9216;
constexpr int L_OI = 65536;
constexpr int L_B = 147456;
constexpr int L_C = 155648;
constexpr int LDS_BYTES = 163840;

#define STAMP(i) do {} while (0)
#define STAMPW(i) do {} while (0)

#define SCHED() __builtin_amdgcn_sched_barrier(0)
#define UNTIL(t) do { _Pragma("nounroll") while ((int)((u32)__builtin_amdgcn_s_memrealtime() - (t)) < 0) __builtin_amdgcn_s_sleep(1); asm volatile("" ::: "memory"); } while (0)
#define BAR() asm volatile("s_barrier" ::: "memory")
#define TS(i) do {} while (0)
#define TSOUT() do {} while (0)
#define TSA(i) do {} while (0)
#define TSB(i) do {} while (0)
#define TSC(i) do {} while (0)
#define WAITV_(n) do { asm volatile("s_waitcnt vmcnt(" #n ")" ::: "memory"); SCHED(); } while (0)
#define WAITV(n) WAITV_(n)
#define DK 5
#define DV DK
#define PR 117
#define LF 60
#define TX 160
#define H3 4
#define UA 5
#define NB 4
#define HD3 70
#define H1 2
#define H2 5
#define HD2 15
#define HS2 0
#define YD 150
#define TADJ -10
#define SLOWJ 6
#define TGO 210

__device__ __forceinline__ float lo16(u32 u) { return __uint_as_float(u << 16); }
__device__ __forceinline__ float hi16(u32 u) { return __uint_as_float(u & 0xffff0000u); }
__device__ __forceinline__ float b2f(u16 u) { return __uint_as_float((u32)u << 16); }
__device__ __forceinline__ u32 cvt2(float a, float b) { return __builtin_bit_cast(u32, __builtin_convertvector(f32x2{a, b}, bf16x2)); }
__device__ __forceinline__ float rbf(float v) { return __uint_as_float(cvt2(0.f, v)); }
__device__ __forceinline__ u32 sanf(float f) { u32 v = __float_as_uint(f); return v < 0xfffffffeu ? v : 0xfffffffeu; }
__device__ __forceinline__ u32 sanu(u32 v) { return v < 0xfffffffeu ? v : 0xfffffffeu; }
__device__ __forceinline__ f32x4 asf4(u32x4 v) { return __builtin_bit_cast(f32x4, v); }
__device__ __forceinline__ f32x2 up2(u32 u) { return f32x2{lo16(u), hi16(u)}; }
__device__ __forceinline__ f32x2 rbf2(f32x2 v) { return f32x2{rbf(v.x), rbf(v.y)}; }
__device__ __forceinline__ void lds_barrier() { asm volatile("s_waitcnt lgkmcnt(0)\n\ts_barrier" ::: "memory"); }
__device__ __forceinline__ f32x4 mfma(u32x4 a, u32x4 b, f32x4 c) {
  return __builtin_amdgcn_mfma_f32_16x16x32_bf16(__builtin_bit_cast(bf16x8, a), __builtin_bit_cast(bf16x8, b), c, 0, 0, 0);
}
__device__ __forceinline__ u32 plo(u32 a, u32 b) { return __builtin_amdgcn_perm(b, a, 0x05040100u); }
__device__ __forceinline__ u32 phi(u32 a, u32 b) { return __builtin_amdgcn_perm(b, a, 0x07060302u); }

extern "C" __device__ __attribute__((const)) unsigned long long llvm_dispatch_id() __asm("llvm.amdgcn.dispatch.id");
typedef __amdgpu_buffer_rsrc_t rsrc_t;
__device__ __forceinline__ u32x4 ld128(rsrc_t rs, int off) { return __builtin_amdgcn_raw_buffer_load_b128(rs, off, 0, 16); }
__device__ __forceinline__ u32 ld32(rsrc_t rs, int off) { return __builtin_amdgcn_raw_buffer_load_b32(rs, off, 0, 16); }
__device__ __forceinline__ void st128(rsrc_t rs, int off, u32x4 v) { __builtin_amdgcn_raw_buffer_store_b128(v, rs, off, 0, 16); }
__device__ __forceinline__ void st64(rsrc_t rs, int off, u32x2 v) { __builtin_amdgcn_raw_buffer_store_b64(v, rs, off, 0, 16); }
__device__ __forceinline__ void dma(const void* gbase, u32 voff, unsigned lds_dst) {
  unsigned keep;
  asm volatile("s_mov_b32 %0, m0\n\ts_mov_b32 m0, %3\n\ts_nop 2\n\tglobal_load_lds_dwordx4 %1, %2 nt\n\ts_mov_b32 m0, %0"
               : "=&s"(keep) : "v"(voff), "s"(gbase), "s"(lds_dst) : "memory");
}
__device__ __forceinline__ void dma_keep(const void* gbase, u32 voff, unsigned lds_dst) {
  unsigned keep;
  asm volatile("s_mov_b32 %0, m0\n\ts_mov_b32 m0, %3\n\ts_nop 2\n\tglobal_load_lds_dword %1, %2\n\ts_mov_b32 m0, %0"
               : "=&s"(keep) : "v"(voff), "s"(gbase), "s"(lds_dst) : "memory");
}
__device__ __forceinline__ void dma_keep64(const void* addr, unsigned lds_dst) {
  unsigned keep;
  asm volatile("s_mov_b32 %0, m0\n\ts_mov_b32 m0, %2\n\ts_nop 2\n\tglobal_load_lds_dword %1, off\n\ts_mov_b32 m0, %0"
               : "=&s"(keep) : "v"(addr), "s"(lds_dst) : "memory");
}
__device__ __forceinline__ void dma_sc(const void* gbase, u32 voff, unsigned lds_dst) {
  unsigned keep;
  asm volatile("s_mov_b32 %0, m0\n\ts_mov_b32 m0, %3\n\ts_nop 2\n\tglobal_load_lds_dwordx4 %1, %2 sc1\n\ts_mov_b32 m0, %0"
               : "=&s"(keep) : "v"(voff), "s"(gbase), "s"(lds_dst) : "memory");
}
__device__ __forceinline__ void ald128(u32x4& d, const void* gbase, u32 voff) { unsigned long long t; asm volatile("s_mov_b64 %1, %3\n\tglobal_load_dwordx4 %0, %2, %1" : "=v"(d), "=&s"(t) : "v"(voff), "s"(gbase) : "memory"); }
__device__ __forceinline__ void ald64(u32x2& d, const void* gbase, u32 voff) { unsigned long long t; asm volatile("s_mov_b64 %1, %3\n\tglobal_load_dwordx2 %0, %2, %1" : "=v"(d), "=&s"(t) : "v"(voff), "s"(gbase) : "memory"); }
__device__ __forceinline__ void ald128sc(u32x4& d, const void* gbase, u32 voff) { unsigned long long t; asm volatile("s_mov_b64 %1, %3\n\tglobal_load_dwordx4 %0, %2, %1 sc1" : "=v"(d), "=&s"(t) : "v"(voff), "s"(gbase) : "memory"); }
__device__ __forceinline__ u32x2 trd(const unsigned char* lds, int off) {
  return __builtin_bit_cast(u32x2, __builtin_amdgcn_ds_read_tr16_b64_v4i16((__attribute__((address_space(3))) s16x4*)((lds_t*)lds + off)));
}
#define nap(n) do { __builtin_amdgcn_s_sleep(n); asm volatile("" ::: "memory"); } while (0)
__device__ __forceinline__ float frcp(float v) { return __builtin_amdgcn_rcpf(v); }
__device__ __forceinline__ float fdiv(float a, float d, float r) { const float q = a * r; return fmaf(fmaf(-q, d, a), r, q); }
__device__ __forceinline__ float silu(float v) { const float d = 1.f + __expf(-v); const float r = frcp(d); return d < 3.0e38f ? fdiv(v, d, r) : v * r; }
template <int CTRL> __device__ __forceinline__ float dppf(float x) {
  return __builtin_bit_cast(float, __builtin_amdgcn_mov_dpp(__builtin_bit_cast(int, x), CTRL, 0xf, 0xf, true));
}
template <int CTRL> __device__ __forceinline__ u32 dppu(u32 x) { return (u32)__builtin_amdgcn_mov_dpp((int)x, CTRL, 0xf, 0xf, true); }
__device__ __forceinline__ u32 mxu(u32 a, u32 b) { return a > b ? a : b; }
__device__ __forceinline__ u32 mx4(u32x4 v) { return mxu(mxu(v[0], v[1]), mxu(v[2], v[3])); }
__device__ __forceinline__ float rl(float v, int l) { return __builtin_bit_cast(float, __builtin_amdgcn_readlane(__builtin_bit_cast(int, v), l)); }
__device__ __forceinline__ float wsum(float v) {
  v += dppf<0xB1>(v); v += dppf<0x4E>(v); v += dppf<0x141>(v); v += dppf<0x140>(v);
  return (rl(v, 0) + rl(v, 16)) + (rl(v, 32) + rl(v, 48));
}
__device__ __forceinline__ float up32(float v) { const u32 u = __float_as_uint(v); return __uint_as_float(__builtin_amdgcn_permlane32_swap(u, u, false, false)[1]); }
__device__ __forceinline__ float xsum32(float v) { const u32 u = __float_as_uint(v); const auto r = __builtin_amdgcn_permlane32_swap(u, u, false, false); return __uint_as_float(r[0]) + __uint_as_float(r[1]); }
__device__ __forceinline__ float xsum16(float v) { const u32 u = __float_as_uint(v); const auto r = __builtin_amdgcn_permlane16_swap(u, u, false, false); return __uint_as_float(r[0]) + __uint_as_float(r[1]); }
__device__ __forceinline__ float xmax32(float v) { const u32 u = __float_as_uint(v); const auto r = __builtin_amdgcn_permlane32_swap(u, u, false, false); return fmaxf(__uint_as_float(r[0]), __uint_as_float(r[1])); }
__device__ __forceinline__ float xmax16(float v) { const u32 u = __float_as_uint(v); const auto r = __builtin_amdgcn_permlane16_swap(u, u, false, false); return fmaxf(__uint_as_float(r[0]), __uint_as_float(r[1])); }
__device__ __forceinline__ float red8(f32x4 v) {
  const auto a = __builtin_amdgcn_permlane32_swap(__float_as_uint(v[0]), __float_as_uint(v[2]), false, false);
  const auto b = __builtin_amdgcn_permlane32_swap(__float_as_uint(v[1]), __float_as_uint(v[3]), false, false);
  const float w0 = __uint_as_float(a[0]) + __uint_as_float(a[1]), w1 = __uint_as_float(b[0]) + __uint_as_float(b[1]);
  const auto c = __builtin_amdgcn_permlane16_swap(__float_as_uint(w0), __float_as_uint(w1), false, false);
  const float z = __uint_as_float(c[0]) + __uint_as_float(c[1]);
  return z + dppf<0x128>(z);
}
__device__ __forceinline__ u32x2 gather4(float z) {
  const u32 u = __float_as_uint(z);
  const auto a = __builtin_amdgcn_permlane16_swap(u, u, false, false);
  const u32 pk = phi(a[0], a[1]);
  const auto b = __builtin_amdgcn_permlane32_swap(pk, pk, false, false);
  return u32x2{b[0], b[1]};
}
__device__ __forceinline__ f32x4 half_sum(f32x4 d) {
  f32x4 r;
#pragma unroll
  for (int j = 0; j < 4; ++j) r[j] = d[j] + dppf<0x108>(up32(d[j]));
  return r;
}
__device__ __forceinline__ float half_sum1(f32x4 d, bool lo) {
  const auto a = __builtin_amdgcn_permlane32_swap(__float_as_uint(d[0]), __float_as_uint(d[2]), false, false);
  const auto b = __builtin_amdgcn_permlane32_swap(__float_as_uint(d[1]), __float_as_uint(d[3]), false, false);
  return __uint_as_float(lo ? a[0] : b[1]) + dppf<0x128>(__uint_as_float(lo ? b[0] : a[1]));
}
__device__ __forceinline__ void unpack8(u32x4 u, float* f) {
#pragma unroll
  for (int j = 0; j < 4; ++j) { f[2 * j] = lo16(u[j]); f[2 * j + 1] = hi16(u[j]); }
}
__device__ __forceinline__ void ln_row(const float (&v)[16], bool has2, float& mean, float& den) {
  float s = 0.f;
#pragma unroll
  for (int i = 0; i < 8; ++i) s += v[i];
  if (has2) {
#pragma unroll
    for (int i = 8; i < 16; ++i) s += v[i];
  }
  s = wsum(s);
  const float mu = s * (1.f / D);
  mean = rbf(mu);
  float q = 0.f;
#pragma unroll
  for (int i = 0; i < 8; ++i) { const float t = v[i] - mu; q = fmaf(t, t, q); }
  if (has2) {
#pragma unroll
    for (int i = 8; i < 16; ++i) { const float t = v[i] - mu; q = fmaf(t, t, q); }
  }
  q = wsum(q);
  const float var = rbf(q * (1.f / D));
  den = rbf(sqrtf(rbf(var + 1e-5f)));
}
__device__ __forceinline__ float pick2(u32 t, int e) { return (e & 1) ? hi16(t) : lo16(t); }
__device__ __forceinline__ void ln9(u32x4 x8, float v8, const float (&cb1)[9], const float (&sb)[9], u32x4& h8, u32& h1) {
  f32x2 v[4];
#pragma unroll
  for (int i = 0; i < 4; ++i) v[i] = up2(x8[i]);
  const f32x2 s2 = (v[0] + v[1]) + (v[2] + v[3]);
  const float mu = wsum((s2.x + s2.y) + v8) * (1.f / D);
  const f32x2 mu2 = {mu, mu};
  const float t8 = v8 - mu;
  f32x2 q = {t8 * t8, 0.f};
#pragma unroll
  for (int i = 0; i < 4; ++i) { const f32x2 t = v[i] - mu2; q = __builtin_elementwise_fma(t, t, q); }
  const float mean = rbf(mu);
  f32x2 d[4];
#pragma unroll
  for (int i = 0; i < 4; ++i) d[i] = rbf2(v[i] - f32x2{mean, mean});
  const float d8 = rbf(v8 - mean);
  const float var = rbf(wsum(q.x + q.y) * (1.f / D));
  const float rden = frcp(rbf(__builtin_amdgcn_sqrtf(rbf(var + 1e-5f))));
#pragma unroll
  for (int i = 0; i < 4; ++i) {
    const f32x2 t = rbf2(rbf2(d[i] * f32x2{rden, rden}) * f32x2{cb1[2 * i], cb1[2 * i + 1]}) + f32x2{sb[2 * i], sb[2 * i + 1]};
    h8[i] = cvt2(t.x, t.y);
  }
  h1 = cvt2(rbf(rbf(d8 * rden) * cb1[8]) + sb[8], 0.f);
}
template <int J0 = 0, int J1 = 9>
__device__ __forceinline__ void slab_dma(const u16* base, int stride_b, unsigned lds_dst, int wave, int lane) {
  const u32 v0 = (lane >> 3) * stride_b + (((lane & 7) ^ ((wave & 1) * 4 + (lane >> 4))) << 4), v1 = v0 ^ 32u;
  const u32 v4 = wave >= 4 ? v1 : v0;
  const char* gb = (const char*)base + (size_t)wave * 8 * stride_b;
#pragma unroll
  for (int j = J0; j < J1; ++j) dma(gb + (size_t)j * 64 * stride_b, j < 4 ? v0 : j == 4 ? v4 : v1, lds_dst + (j * 8 + wave) * 1024);
}
__device__ __forceinline__ void slab_frags(const unsigned char* lds, int base, int wave, int lane, u32x4 (&wf)[9]) {
  const int g = lane >> 4, q = (lane & 15) >> 2, p = lane & 3, hf = p >> 1;
  const int a0 = base + (hf * 288 + 8 * g + q) * 128 + ((wave ^ ((g & 1) * 4 + (q >> 1)) ^ (hf * 2)) << 4) + (p & 1) * 8, a1 = (a0 ^ 32) + 512;
#pragma unroll
  for (int s = 0; s < 9; ++s) {
    const u32x2 lo = trd(lds, a0 + s * 4096), hi = trd(lds, a1 + s * 4096);
    wf[s] = u32x4{lo[0], lo[1], hi[0], hi[1]};
  }
}
__device__ __forceinline__ void img_frags(const unsigned char* lds, int img, int lane, u32x4 (&af)[9]) {
#pragma unroll
  for (int s = 0; s < 9; ++s) af[s] = *(const u32x4*)(lds + img + (s * 64 + lane) * 16);
}
__device__ __forceinline__ f32x4 gemv9(const u32x4 (&wf)[9], const u32x4 (&af)[9]) {
  f32x4 acc[3];
  SCHED();
#pragma unroll
  for (int s = 0; s < 9; ++s) acc[s % 3] = mfma(wf[s], af[s], s < 3 ? f32x4{0.f, 0.f, 0.f, 0.f} : acc[s % 3]);
  return (acc[0] + acc[1]) + acc[2];
}
__device__ __forceinline__ int img_off(int b, int k8) {
  const int hf = k8 >= 36 ? 1 : 0, kk = k8 - hf * 36;
  return ((kk >> 2) * 64 + (kk & 3) * 16 + hf * 8 + b) * 16;
}
__device__ __forceinline__ u32 tag_x(u32x4 v, u32 e0, u32 e1) { return (v[2] ^ e0) | (v[3] ^ e1); }
__device__ __forceinline__ bool tag_ok(u32x4 v, u32 e0, u32 e1) { return tag_x(v, e0, e1) == 0u; }

constexpr int CODE_BYTES = 32000;

__global__ void __launch_bounds__(NT, 2) fused_kernel(const u16* __restrict__ x, const u16* __restrict__ kc, const u16* __restrict__ vc,
                                                      const u16* __restrict__ wq, const u16* __restrict__ wo, const u16* __restrict__ w_r,
                                                      const u16* __restrict__ wg, const u16* __restrict__ wu, const u16* __restrict__ wd,
                                                      const u16* __restrict__ c, const u16* __restrict__ w_mod, unsigned char* ws, u16* out) {
  __shared__ __attribute__((aligned(16))) unsigned char lds[LDS_BYTES];
  int w = blockIdx.x;
  const int tid = threadIdx.x, lane = tid & 63, wave = __builtin_amdgcn_readfirstlane(tid >> 6);
  const int n = lane & 15, grp = lane >> 4;
  const size_t pairbase = ((size_t)(w >> 1) * SK + (SK - WIN)) * 256 + (w & 1) * 16384;
  const int wpart = (wave & 3) * 4096 + (wave >> 2) * 262144;
  const int ko = wpart + (n >> 2) * 256 + grp * 64 + (n & 3) * 16;
  const char* kp = (const char*)kc + pairbase;
  u32x4 kb[32], vb[32];
#define KLD(u) do { unsigned long long t_; asm volatile("s_mov_b64 %1, %3\n\tglobal_load_dwordx4 %0, %2, %1 offset:%4 nt" : "=v"(kb[u]), "=&s"(t_) : "v"(ko), "s"(kp + ((u) >> 2) * 32768), "i"(((u) & 3) * 1024) : "memory"); } while (0)
  unsigned long long tst;
  asm volatile("s_memrealtime %0\n\tglobal_load_dwordx4 %1, %5, %6 nt\n\tglobal_load_dwordx4 %2, %5, %6 offset:1024 nt\n\t"
               "global_load_dwordx4 %3, %5, %6 offset:2048 nt\n\tglobal_load_dwordx4 %4, %5, %6 offset:3072 nt"
               : "=&s"(tst), "=&v"(kb[0]), "=&v"(kb[1]), "=&v"(kb[2]), "=&v"(kb[3]) : "v"(ko), "s"(kp) : "memory");
  asm volatile("s_waitcnt lgkmcnt(0)" : "+s"(w), "+s"(ws), "+s"(tst));
  const u32 tstart = (u32)tst;
  const int xcd = w & 7, idx = w >> 3;
  const rsrc_t rs = __builtin_amdgcn_make_buffer_rsrc(ws, 0, W_END, 0x00020000);
  const unsigned lds_base = (unsigned)(uintptr_t)(lds_t*)lds;
  const unsigned long long epoch = (llvm_dispatch_id() + 1ull) * 0x9E3779B97F4A7C15ull;
  const u32 e0 = (u32)epoch, e1 = (u32)(epoch >> 32);
  unsigned long long pc0; asm volatile("s_getpc_b64 %0" : "=s"(pc0));
  asm volatile("" : "+s"(vc), "+s"(w_r), "+s"(wu), "+s"(wd), "+s"(out), "+s"(x), "+s"(w_mod), "+s"(wg), "+s"(wq), "+s"(wo));

  const int mw = idx < 6 ? idx * 8 + xcd : -1;
  const int md = mw < 18 ? mw : -1;
  const int me = mw >= 18 ? mw : -1;
  const int ml = mw >= 0 && mw < 6 ? 48 + mw : -1;
  const int qs = idx >= 6 && idx < 14 ? xcd * 8 + idx - 6 : -1;
  const int oj = idx >= 14 && idx < 23 ? idx - 14 : -1, op = xcd;
  const int gs = idx >= 23 ? xcd * 9 + idx - 23 : -1;

  const int s_att = w & 1, kvh = (w >> 1) & 15, b_att = w >> 5;
  {
    const char* vp = (const char*)vc + pairbase;
    const int vo = wpart + lane * 16;
#define VLD(u) do { unsigned long long t_; asm volatile("s_mov_b64 %1, %3\n\tglobal_load_dwordx4 %0, %2, %1 offset:%4 nt" : "=v"(vb[u]), "=&s"(t_) : "v"(vo), "s"(vp + ((u) >> 2) * 32768), "i"(((u) & 3) * 1024) : "memory"); } while (0)
#define TIE8(a, o) asm volatile("" : "+v"(a[o]), "+v"(a[(o) + 1]), "+v"(a[(o) + 2]), "+v"(a[(o) + 3]), "+v"(a[(o) + 4]), "+v"(a[(o) + 5]), "+v"(a[(o) + 6]), "+v"(a[(o) + 7]))
    constexpr int pr_ = PR;
    const int tx_ = gs >= 0 ? TX - LF : TX;
    const u32 tp0 = tstart + tx_ - TADJ;
#define PACE(i) do { _Pragma("nounroll") while ((int)((u32)__builtin_amdgcn_s_memrealtime() - tp0) < (i) * pr_ / 4) __builtin_amdgcn_s_sleep(2); SCHED(); } while (0)
#define REP4(M, u, W) do { M(u); W; M((u) + 1); W; M((u) + 2); W; M((u) + 3); W; } while (0)
    static_assert(DK <= 7 && DV <= 7, "the hand-placed waits assume it");
#define POISON() do { if (gs >= 0) { const int g_ = gs * PZ + tid; \
      if (tid < PZ && g_ < W_MS / 16) st128(rs, g_ * 16, u32x4{0xffffffffu, 0xffffffffu, 0xffffffffu, 0xffffffffu}); } SCHED(); } while (0)
#define PFLAG() do { if (gs >= 0 && tid == 448) st64(rs, W_FL + gs * 8, u32x2{e0, e1}); SCHED(); } while (0)
#define FOK(a) __all(((a[0] ^ e0) | (a[1] ^ e1) | tag_x(a, e0, e1)) == 0u)
#define FREQ() do { if (wave == 1) dma_sc(ws + W_FL, min(lane, NG / 2 - 1) * 16, lds_base + L_B + 2048); } while (0)
#define FCHK() do { if (wave == 1 && !FOK((*(const u32x4*)(lds + L_B + 2048 + lane * 16)))) { \
      _Pragma("nounroll") for (int sp = 0; sp < SPIN_CAP; ++sp) { nap(4); const u32x4 a_ = ld128(rs, W_FL + min(lane, NG / 2 - 1) * 16); if (FOK(a_)) break; } } SCHED(); } while (0)
#define SILU_IMAGE(c0, c1) do { float f[8]; unpack8(c0, f); u32x4 o; \
        _Pragma("unroll") for (int j = 0; j < 4; ++j) o[j] = cvt2(silu(f[2 * j]), silu(f[2 * j + 1])); \
        const int b_ = tid / 72, ch_ = tid - b_ * 72; \
        *(u32x4*)(lds + L_IMG + img_off(b_, ch_)) = o; \
        if (wave == 0) { unpack8(c1, f); \
          _Pragma("unroll") for (int j = 0; j < 4; ++j) o[j] = cvt2(silu(f[2 * j]), silu(f[2 * j + 1])); \
          *(u32x4*)(lds + L_IMG + img_off(7, 8 + lane)) = o; } } while (0)
    if (md >= 0) {
      u32x4 c0, c1, x0, x1; u32x2 xc;
      TSA(0);
      int ho = W_H + img_off(n & 7, (md >= 9 ? md - 9 : md) * 8 + wave) * 2 + (grp & 1) * 16;
      ald128(c0, c, tid * 16);
      ald128(c1, c, (512 + lane) * 16);
      ald128(x0, x, wave * 1152 + lane * 16);
      ald128(x1, x, wave * 1152 + 1024 + (lane & 7) * 16);
      ald64(xc, x, (n & 7) * 1152 + ((md >= 9 ? md - 9 : 0) * 64 + wave * 8 + (grp & 1) * 4) * 2);
      BAR();
      float* st = (float*)(lds + L_TILE + 1024);
      if (wave < 4) {
        slab_dma(w_mod + md * 64, NMOD * 2, lds_base + L_W, wave, lane);
        asm volatile("s_waitcnt vmcnt(9)" ::: "memory");
      } else asm volatile("s_waitcnt vmcnt(0)" ::: "memory");
      asm volatile("s_nop 0 ; both branches above cover V" : "+v"(c0), "+v"(c1), "+v"(x0), "+v"(x1), "+v"(xc) :: "memory");
      TSA(1);
      {
        SILU_IMAGE(c0, c1);
        float v[16]; unpack8(x0, v); unpack8(x1, v + 8);
        float mean, den; ln_row(v, lane < 8, mean, den);
        if (lane == 0) { st[wave] = mean; st[8 + wave] = den; }
      }
      TSA(2);
      if (wave >= 4) slab_dma(w_mod + md * 64, NMOD * 2, lds_base + L_W, wave, lane);
      TSA(3);
      lds_barrier();
      REP4(KLD, 4, );
      u32x4 wf[9], af[9];
      img_frags(lds, L_IMG, lane, af);
      const float mean = st[n & 7], rd = frcp(st[8 + (n & 7)]);
      float ln[4] = {rbf(rbf(lo16(xc[0]) - mean) * rd), rbf(rbf(hi16(xc[0]) - mean) * rd), rbf(rbf(lo16(xc[1]) - mean) * rd), rbf(rbf(hi16(xc[1]) - mean) * rd)};
#pragma unroll
      for (int s = 0; s < 9; ++s) asm volatile("" : "+v"(af[s]));
      asm volatile("" : "+v"(ln[0]), "+v"(ln[1]), "+v"(ln[2]), "+v"(ln[3]), "+v"(ho));
      WAITV(4); BAR(); SCHED();
      TSA(5);
      slab_frags(lds, L_W, wave, lane, wf);
      const f32x4 s = half_sum(gemv9(wf, af));
      STAMP(14); TSA(6);
      f32x4 r = s;
      if (md >= 9) {
#pragma unroll
        for (int j = 0; j < 4; ++j) r[j] = ln[j] * rbf(1.f + rbf(s[j]));
      }
      if (n < 8 && grp < 2) st128(rs, ho + (md >= 9 ? 18432 : 0), u32x4{cvt2(r[0], r[1]), cvt2(r[2], r[3]), e0, e1});
      SCHED();
      STAMP(15); TSA(7);
      BAR();
      KLD(8); KLD(9); WAITV(DK); KLD(10); WAITV(DK);
      KLD(11); WAITV(DK); REP4(KLD, 12, WAITV(DK)); REP4(KLD, 16, WAITV(DK));
      FREQ(); REP4(KLD, 20, WAITV(DK)); PACE(24); REP4(KLD, 24, WAITV(DK)); PACE(28); KLD(28); WAITV(DK); KLD(29);
      FCHK();
    } else {
      constexpr int L_LAND = 98304;
      const unsigned land = lds_base + L_LAND + wave * 6144;
      const unsigned char* lp = lds + L_LAND + wave * 6144 + lane * 16;
      const int sl_ = (3 - (int)((uintptr_t)wq >> 7)) & 7, sm_ = (63 - (int)((uintptr_t)wq >> 7)) & 63;
      const bool slowq = qs >= 0 && ((qs & 7) == sl_ || qs == sm_);
      if (oj >= 0 && wave < 2) {
        if (wave == 0 || (sm_ >> 3) == xcd) dma_keep((const char*)wq + (size_t)oj * 64 * 8192 + (wave == 0 ? xcd * 8 + sl_ : sm_) * 128, lane * 8192, land);
      }
      POISON();
      if (oj >= 0 && oj < 8 && wave == 4)
        dma_keep((const void*)pc0, min((oj & 3) * 8192 + lane * 128, CODE_BYTES - 128), land);
      const u32 v0 = (lane >> 3) * 1152 + (((lane & 7) ^ ((lane >> 4) & 1)) << 4);
      const char* gb = (const char*)(wo + (size_t)(op * 512 + wave * 64) * D + oj * 64);
      if (me >= 0) { dma_sc(c, tid * 16, land); dma_sc(c, (512 + lane) * 16, land + 1024); }
      const bool fs = qs >= 0 || me >= 0;
      const u16* sbase = qs >= 0 ? wq + qs * 64 : w_mod + me * 64;
      const int sstr = qs >= 0 ? NQ * 2 : NMOD * 2;
      const unsigned sdst = lds_base + L_W;
#define WK(j) do { if (!fs) { UNTIL(tp0 + ((j) + 4) * pr_ / 4 - (gs >= 0 ? (TX - LF) * (9 - (j)) / 13 : TX * (9 - (j)) / 13)); \
                     if (oj >= 0 && ((j) & 1) && NB + (j) / 2 < 8) dma(gb + (NB + (j) / 2) * 8 * 1152, v0 ^ (((NB + (j) / 2) & 1) * 32 + ((NB + (j) / 2) >> 2) * 64), lds_base + L_W + (wave * 8 + NB + (j) / 2) * 1024); } \
                   else if (slowq) { if ((j) == SLOWJ) slab_dma(sbase, sstr, sdst, wave, lane); } \
                   else slab_dma<j, (j) + 1>(sbase, sstr, sdst, wave, lane); \
                   KLD((j) + 4); WAITV(DK); } while (0)
      WK(0); WK(1); WK(2); WK(3);
      WK(4); WK(5);
      static_assert(DK <= 5, "six younger requests cover the poison");
      if (me >= 0) { const u32x4 c0 = *(const u32x4*)lp, c1 = *(const u32x4*)(lp + 1024); SILU_IMAGE(c0, c1); }
      BAR();
      STAMP(2);
      PFLAG();
      WK(6); WK(7); WK(8);
      PACE(13); KLD(13); WAITV(DK); KLD(14); WAITV(DK); KLD(15); WAITV(DK);
      const int hg0 = (wave * 144 + lane) * 16, hg2 = (wave * 144 + 128 + n) * 16;
#define HREQ() do { dma_sc(ws + W_H, hg0, land); dma_sc(ws + W_H + 18432, hg0, land + 1024); dma_sc(ws + W_H + 1024, hg0, land + 2048); \
                    dma_sc(ws + W_H + 18432 + 1024, hg0, land + 3072); dma_sc(ws + W_H, hg2, land + 4096); dma_sc(ws + W_H + 18432, hg2, land + 5120); } while (0)
#define HGET() do { _Pragma("unroll") for (int i = 0; i < 6; ++i) hq[i] = *(const u32x4*)(lp + i * 1024); } while (0)
#define HQOK() __all((tag_x(hq[0], e0, e1) | tag_x(hq[1], e0, e1) | tag_x(hq[2], e0, e1) | tag_x(hq[3], e0, e1) | tag_x(hq[4], e0, e1) | tag_x(hq[5], e0, e1)) == 0u)
      PACE(16); KLD(16); WAITV(DK); KLD(17); WAITV(DK); PACE(18); KLD(18); WAITV(DK); KLD(19); WAITV(DK);
      TSA(4);
      if (qs >= 0) HREQ();
      PACE(20); FREQ(); KLD(20); WAITV(DK); KLD(21); WAITV(DK); PACE(22); KLD(22); WAITV(DK); KLD(23); WAITV(DK);
      if (qs >= 0) WAITV(4);
      TSA(5);
      PACE(24); KLD(24); KLD(25);
      if (qs >= 0) {
        u32x4 hq[6]; HGET();
        _Pragma("nounroll") for (int sp = 0; sp < SPIN_CAP && !HQOK(); ++sp) { nap(16); HREQ(); WAITV(0); HGET(); }
        STAMP(14); TSA(6);
#define HSUM(a, b) cvt2(lo16(a) + lo16(b), hi16(a) + hi16(b))
        *(u32x2*)(lds + L_IMG + hg0 / 2) = u32x2{HSUM(hq[0][0], hq[1][0]), HSUM(hq[0][1], hq[1][1])};
        *(u32x2*)(lds + L_IMG + 512 + hg0 / 2) = u32x2{HSUM(hq[2][0], hq[3][0]), HSUM(hq[2][1], hq[3][1])};
        if (lane < 16) *(u32x2*)(lds + L_IMG + hg2 / 2) = u32x2{HSUM(hq[4][0], hq[5][0]), HSUM(hq[4][1], hq[5][1])};
      } else WAITV(DK);
      KLD(26); KLD(27);
      if (fs) {
        u32x4 wf[9], af[9];
        lds_barrier();
        slab_frags(lds, L_W, wave, lane, wf);
        img_frags(lds, L_IMG, lane, af);
        const f32x4 s = half_sum(gemv9(wf, af));
        if (n < 8 && grp < 2) *(u32x2*)(lds + L_TILE + n * 128 + wave * 16 + grp * 8) = u32x2{cvt2(s[0], s[1]), cvt2(s[2], s[3])};
      } else WAITV(DK);
      PACE(28); KLD(28); KLD(29);
      WAITV(9); FCHK();
#define MPUB(m_) do { if (wave == 0) { const u32x4 t_ = *(const u32x4*)(lds + L_TILE + lane * 16); const int ch_ = ((m_) - 18) / 9, sl_2 = (m_) - 18 - ch_ * 9; \
          st128(rs, W_M + (ch_ * 8 + (lane >> 3)) * 1152 + sl_2 * 128 + (lane & 7) * 16, u32x4{sanu(t_[0]), sanu(t_[1]), sanu(t_[2]), sanu(t_[3])}); } } while (0)
      if (fs) {
        lds_barrier();
        if (qs < 0) MPUB(me);
        else if (wave < 2) {
          const u32x2 t2 = *(const u32x2*)(lds + L_TILE + tid * 8);
          st128(rs, W_Q + qs * 256 + (tid >> 4) * 16384 + n * 16, u32x4{t2[0], t2[1], e0, e1});
        }
        STAMP(15); TSA(7);
      }
      SCHED();
    }
    STAMP(3); TSB(0);
    const unsigned char* qg = ws + W_Q + (b_att * 1024 + kvh * 64) * 16;
    PACE(30); KLD(30); WAITV(DK); KLD(31); WAITV(DK);
    TSB(1);
    PACE(32); VLD(0); WAITV(DK); VLD(1); WAITV(DK); PACE(34); VLD(2); WAITV(DK); VLD(3); WAITV(DK);
    PACE(36); VLD(4); WAITV(DK); VLD(5); WAITV(DK);
    if (wave == 1) dma_sc(qg, lane * 16, lds_base + L_B + 1024);
    PACE(38); VLD(6); WAITV(DK); VLD(7); WAITV(DK);
    PACE(40); VLD(8); WAITV(DV); VLD(9); WAITV(DV);
    if (wave == 1) dma_sc(qg, lane * 16, lds_base + L_B + 1024);
    PACE(42); VLD(10); WAITV(DV); VLD(11); WAITV(DV);
    TIE8(kb, 0); TIE8(kb, 8); TIE8(kb, 16); TIE8(kb, 24);
    PACE(44); VLD(12); VLD(13);
    u32x4 qf[2];
    {
      const u32x4* ql = (const u32x4*)(lds + L_B + 1024 + (((n >> 2) & 1) * 32 + grp * 8 + (n & 3) * 2) * 16);
      u32x4 g[2];
      _Pragma("nounroll") for (int sp = 0; sp < SPIN_CAP; ++sp) {
        g[0] = ql[0]; g[1] = ql[1];
        if (__all((tag_x(g[0], e0, e1) | tag_x(g[1], e0, e1)) == 0u)) break;
        if (wave == 1) { dma_sc(qg, lane * 16, lds_base + L_B + 1024); WAITV(0); } else nap(4);
      }
      asm volatile("" ::: "memory");
      g[0] = ql[0]; g[1] = ql[1];
      qf[0] = n < 8 ? u32x4{g[0][0], g[0][1], g[1][0], g[1][1]} : u32x4{0, 0, 0, 0};
      qf[1] = n < 8 ? u32x4{0, 0, 0, 0} : u32x4{g[0][0], g[0][1], g[1][0], g[1][1]};
    }
    STAMP(4); TSB(2);
    constexpr float SCALE = 0.088388347648318440f;
    float sc[4];
#pragma unroll
    for (int i = 0; i < 4; ++i) {
      float y[2];
#pragma unroll
      for (int t = 0; t < 2; ++t) {
        const int u = 8 * t + 2 * i;
        const f32x4 a = mfma(kb[u + 16], qf[1], mfma(kb[u], qf[0], f32x4{0.f, 0.f, 0.f, 0.f}));
        const f32x4 b = mfma(kb[u + 17], qf[1], mfma(kb[u + 1], qf[0], f32x4{0.f, 0.f, 0.f, 0.f}));
        const float own = (n & 2) ? ((n & 1) ? b[3] : a[2]) : ((n & 1) ? b[1] : a[0]), oth = (n & 2) ? ((n & 1) ? a[3] : b[2]) : ((n & 1) ? a[1] : b[0]);
        y[t] = own + dppf<0xB1>(oth);
      }
      sc[i] = ((n & 2) ? y[1] : y[0]) + dppf<0x4E>((n & 2) ? y[0] : y[1]);
      if (i == 1 || i == 3) { SCHED(); if (i == 1) VLD(14); else VLD(15); SCHED(); }
    }
#pragma unroll
    for (int i = 0; i < 4; ++i) sc[i] = rbf(rbf(sc[i]) * SCALE);
    float mx = fmaxf(fmaxf(sc[0], sc[1]), fmaxf(sc[2], sc[3]));
    mx = fmaxf(mx, dppf<0xB1>(mx)); mx = fmaxf(mx, dppf<0x4E>(mx)); mx = fmaxf(mx, dppf<0x128>(mx));
    mx = xmax32(xmax16(mx));
#pragma unroll
    for (int i = 0; i < 4; ++i) sc[i] = __expf(sc[i] - mx);
    float ls = (sc[0] + sc[1]) + (sc[2] + sc[3]);
    ls += dppf<0xB1>(ls); ls += dppf<0x4E>(ls); ls += dppf<0x128>(ls);
    ls = xsum32(xsum16(ls));
    float* stat = (float*)(lds + L_B);
    if (tid == 0) *(u32*)(lds + L_B + 512) = 0u;
    if (lane == 0 || lane == 4) *(f32x2*)(stat + (wave * 2 + (lane >> 2)) * 2) = f32x2{mx, ls};
    SCHED(); PACE(48); VLD(16); VLD(17); SCHED();
    TSB(3);
    lds_barrier();
    TSB(4);
    float M = -INFINITY, L = 0.f;
    {
      f32x2 t[8];
#pragma unroll
      for (int q = 0; q < 8; ++q) { t[q] = *(const f32x2*)(stat + (q * 2 + ((n >> 2) & 1)) * 2); M = fmaxf(M, t[q].x); }
#pragma unroll
      for (int q = 0; q < 8; ++q) L = fmaf(__expf(t[q].x - M), t[q].y, L);
    }
    if (tid < 8) st128(rs, W_ST + (w * 8 + lane) * 16, u32x4{__float_as_uint(M), __float_as_uint(L), e0, e1});
    SCHED();
#define VTR(G) do { u32x4 f_[8]; _Pragma("unroll") for (int t = 0; t < 8; ++t) _Pragma("unroll") for (int j = 0; j < 4; ++j) \
      f_[t][j] = (t & 1) ? phi(vb[(G) * 8 + 2 * j][t >> 1], vb[(G) * 8 + 2 * j + 1][t >> 1]) : plo(vb[(G) * 8 + 2 * j][t >> 1], vb[(G) * 8 + 2 * j + 1][t >> 1]); \
      _Pragma("unroll") for (int t = 0; t < 8; ++t) { vb[(G) * 8 + t] = f_[t]; asm volatile("" : "+v"(vb[(G) * 8 + t])); } SCHED(); } while (0)
    WAITV(DV);
    TIE8(vb, 0);
    VTR(0);
    if (gs >= 0) {
      const unsigned char* mb = ws + W_M + (8 + wave) * 1152;
      const unsigned ld_ = lds_base + L_W + wave * 2304;
      dma_sc(mb, lane * 16, ld_); dma_sc(mb + 9216, lane * 16, ld_ + 1024);
      if (lane < 16) dma_sc(mb + 1024, (lane >> 3) * 9216 + (lane & 7) * 16, ld_ + 2048);
    }
    PACE(50); VLD(18); WAITV(DV); VLD(19); WAITV(DV);
    PACE(52); VLD(20); WAITV(5);
    TIE8(vb, 8);
    VTR(1);
    VLD(21); WAITV(DV);
    const int po = W_ST + ((w ^ 1) * 8 + (n & 4)) * 16;
    PACE(54);
    VLD(22); WAITV(DV); VLD(23); WAITV(DV);
    u32x4 ps;
    ald128sc(ps, ws, po);
    PACE(56);
    VLD(24); WAITV(DV); VLD(25); WAITV(DV);
    PACE(58);
    VLD(26); WAITV(DV); VLD(27); WAITV(DV);
    VLD(28); VLD(29); WAITV(6);
    asm volatile("" : "+v"(ps));
    TIE8(vb, 16);
    VLD(30); VLD(31);
    VTR(2);
    if (!__all(tag_ok(ps, e0, e1))) {
      int sp = 0;
      _Pragma("nounroll") do { nap(1); ps = ld128(rs, po); } while (!__all(tag_ok(ps, e0, e1)) && ++sp < SPIN_CAP);
    }
    float fac;
    {
      const float M2 = __uint_as_float(ps[0]), L2 = __uint_as_float(ps[1]);
      const float Mg = fmaxf(M, M2);
      const float Lg = __expf(M - Mg) * L + __expf(M2 - Mg) * L2;
      fac = __expf(mx - Mg) / Lg;
    }
    STAMP(1); TSB(5);
    u32x4 pf[4];
#pragma unroll
    for (int j = 0; j < 4; ++j) {
      const float p_ = sc[j] * fac;
      pf[0][j] = cvt2(p_, dppf<0xB1>(p_));
      pf[1][j] = dppu<0x102>(pf[0][j]); pf[2][j] = dppu<0x108>(pf[0][j]); pf[3][j] = dppu<0x10A>(pf[0][j]);
    }
    f32x4 oa[8];
#pragma unroll
    for (int t = 0; t < 8; ++t) oa[t] = f32x4{0.f, 0.f, 0.f, 0.f};
    SCHED();
#pragma unroll
    for (int G = 0; G < 2; ++G)
#pragma unroll
      for (int t = 0; t < 8; ++t) oa[t] = mfma(pf[G], vb[G * 8 + t], oa[t]);
    SCHED();
#pragma unroll
    for (int t = 0; t < 8; ++t) oa[t] = mfma(pf[2], vb[16 + t], oa[t]);
    SCHED();
    int t2_ = threadIdx.x; asm volatile("" : "+v"(t2_));
    const int tid = t2_, lane = tid & 63, n = lane & 15, grp = lane >> 4;
    int oo = W_O + ((kvh * 8 + b_att) * 2 + s_att) * 1024 + lane * 16;
    int mw_ = L_C + wave * 1024 + lane * 32, mr_ = L_C + (grp * 16 + (lane & 7) * 2 + (n >> 3)) * 16;
    asm volatile("" : "+v"(oo), "+v"(mw_), "+v"(mr_), "+v"(pf[3]));
    u32x4 bg[9], mg[2]; u32 mh[2];
    const u32 dv = gs >= 0 ? (lane >> 3) * 128 + (((lane & 7) ^ ((lane >> 4) & 3)) << 4) : (lane >> 3) * 1152 + (((lane & 7) ^ ((lane >> 4) & 1)) << 4);
    const char* db = gs >= 0 ? (const char*)(wd + (size_t)gs * 64 * D) + wave * 1024 : (const char*)(wo + (size_t)(op * 512 + wave * 64) * D + oj * 64);
    constexpr int NU = 112 * H1 + 78 * (H2 + H3) - 576, NS = (2 * (576 - NU) + 111) / 112;
    static_assert(NU >= 0 && NU < 576 && H1 * 9 + NS <= 64 && H2 * 9 <= 64 && H3 * 9 <= 64, "helper split");
#define HROW(i_) (((i_) < NU ? (const char*)wu + (size_t)(576 - NU) * 9216 : (const char*)wg - (size_t)NU * 9216) + (size_t)(i_) * 9216 + xcd * 1152)
    const char* ha;
    if (gs >= 0) {
#pragma unroll
      for (int i = 0; i < 2; ++i) { mg[i] = *(const u32x4*)(lds + L_W + wave * 2304 + i * 1024 + lane * 16); mh[i] = *(const u32*)(lds + L_W + wave * 2304 + 2048 + i * 128 + (lane >> 1) * 4); }
      lds_barrier();
#pragma unroll
      for (int j = 0; j < 4; ++j) dma(db + j * 8192, dv, lds_base + L_W + 73728 + (j * 8 + wave) * 1024);
      WAITV(6);
    } else if (oj >= 0) {
      BAR();
#pragma unroll
      for (int j = 0; j < NB; ++j) dma(db + j * 8 * 1152, dv ^ ((j & 1) * 32 + (j >> 2) * 64), lds_base + L_W + (wave * 8 + j) * 1024);
      asm volatile("s_waitcnt vmcnt(%0)" :: "n"(NB + 2) : "memory"); SCHED();
    } else {
      const int r_ = max(min(lane / 9, H1 - 1), 0), c_ = lane - (lane / 9) * 9;
      ha = HROW(r_ * 112 + idx * 8 + wave) + c_ * 128;
      if (lane >= H1 * 9) {
        const int k_ = min((idx * 8 + wave) * NS + min(lane - H1 * 9, NS - 1), 2 * (576 - NU) - 1), g0 = ((3 - (int)((uintptr_t)wu >> 7) - xcd * 9) & 7) + (k_ & 1) * 8;
        ha = (const char*)wu + (size_t)(k_ >> 1) * 9216 + (xcd * 9 + (g0 < 9 ? g0 : g0 - 8)) * 128;
      }
      asm volatile("" : "+v"(ha));
      WAITV(2);
    }
    u32x4 vf[8];
    asm volatile("s_nop 0 ; both branches above cover V up to the last two" : "+v"(vb[24]), "+v"(vb[25]), "+v"(vb[26]), "+v"(vb[27]), "+v"(vb[28]), "+v"(vb[29]));
#pragma unroll
    for (int t = 0; t < 8; ++t)
#pragma unroll
      for (int j = 0; j < 3; ++j) vf[t][j] = (t & 1) ? phi(vb[24 + 2 * j][t >> 1], vb[25 + 2 * j][t >> 1]) : plo(vb[24 + 2 * j][t >> 1], vb[25 + 2 * j][t >> 1]);
#pragma unroll
    for (int t = 0; t < 8; ++t) asm volatile("" : "+v"(vf[t][0]), "+v"(vf[t][1]), "+v"(vf[t][2]));
    if (gs >= 0) WAITV(4); else if (oj >= 0) WAITV(NB); else { WAITV(0); dma_keep64(ha, lds_base + L_B + 4096 + wave * 256); }
    asm volatile("s_nop 0 ; both branches above cover V" : "+v"(vb[30]), "+v"(vb[31]));
#pragma unroll
    for (int t = 0; t < 8; ++t) {
      vf[t][3] = (t & 1) ? phi(vb[30][t >> 1], vb[31][t >> 1]) : plo(vb[30][t >> 1], vb[31][t >> 1]);
      oa[t] = mfma(pf[3], vf[t], oa[t]);
    }
    STAMP(5); TSB(6);
    if (grp < 2) {
      *(f32x4*)(lds + mw_) = f32x4{oa[0][0], oa[1][0], oa[2][0], oa[3][0]};
      *(f32x4*)(lds + mw_ + 16) = f32x4{oa[4][0], oa[5][0], oa[6][0], oa[7][0]};
    }
    lds_barrier();
    if (wave == 0) {
      f32x4 m[8];
#pragma unroll
      for (int q = 0; q < 8; ++q) m[q] = *(const f32x4*)(lds + mr_ + q * 1024);
      SCHED();
      const f32x4 o = ((m[0] + m[1]) + (m[2] + m[3])) + ((m[4] + m[5]) + (m[6] + m[7]));
      st128(rs, oo, u32x4{sanf(o[0]), sanf(o[1]), sanf(o[2]), sanf(o[3])});
    }
    STAMP(6); TSB(7); SCHED();
  if (oj >= 0) {
    int wa = L_W + ((lane & 2) * 16 + 8 * grp + (n >> 2)) * 128 + (((n >> 3) ^ ((grp & 1) * 2) ^ ((lane & 2) * 2) ^ wave) << 4) + (lane & 1) * 8;
    int iw = L_OI + wave * 1024 + ((lane & 3) * 16 + ((lane >> 2) & 1) * 8 + ((lane >> 3) ^ (lane & 7))) * 16, ir = L_OI + (lane ^ ((n >> 3) * 4 + grp)) * 16;
    int rw = L_OI + 8192 + (n & 7) * 272 + wave * 32 + (grp & 1) * 16 + (grp >> 1) * 8 + (n >> 3) * 4, rr = L_OI + 8192 + wave * 272 + ((n & 7) * 2 + (n >> 3)) * 16;
    int xo = W_XP + (wave * 9 + oj) * 2048 + (n >> 3) * 1024 + op * 128 + (n & 7) * 16;
    int ob = W_O + ((op * 2 + (wave >> 2)) * 8 + (lane >> 3)) * 2048 + (wave & 3) * 256 + (lane & 7) * 16;
    asm volatile("" : "+v"(rw), "+v"(rr), "+v"(xo), "+v"(ob), "+v"(iw), "+v"(ir), "+v"(wa));
    float xv[2], ga[2]; int xi, xq;
    if (wave == 7) {
      const int g = lane & 7;
      const rsrc_t rx = __builtin_amdgcn_make_buffer_rsrc((void*)x, 0, 8 * D * 2, 0x00020000);
#pragma unroll
      for (int i = 0; i < 2; ++i) xv[i] = b2f(__builtin_amdgcn_raw_buffer_load_b16(rx, (op * D + oj * 64 + g * 8 + i * 4 + grp) * 2, 0, 0));
      u32 mm[2];
      _Pragma("nounroll") for (int sp = 0; sp < SPIN_CAP; ++sp) {
#pragma unroll
        for (int i = 0; i < 2; ++i) mm[i] = ld32(rs, W_M + op * 1152 + oj * 128 + g * 16 + i * 8 + (grp >> 1) * 4);
        if (__all(mxu(mm[0], mm[1]) != 0xffffffffu)) break;
        nap(4);
      }
      ga[0] = pick2(mm[0], grp); ga[1] = pick2(mm[1], grp);
      xi = W_XP + (op * 9 + oj) * 2048 + lane * 16; xq = W_X1 + ((lane >> 3) * 8 + op) * 1152 + oj * 128 + g * 16;
      asm volatile("" : "+v"(xv[0]), "+v"(xv[1]), "+v"(ga[0]), "+v"(ga[1]), "+v"(xi), "+v"(xq));
    }
    if (wave == 0) WAITV(1); else WAITV(0);
    BAR();
    TSC(0);
    u32x4 a[4];
#define OLD() do { _Pragma("unroll") for (int p = 0; p < 4; ++p) a[p] = ld128(rs, ob + (p >> 1) * 1024 + (p & 1) * 128); } while (0)
#define OOK() __all(mxu(mxu(mx4(a[0]), mx4(a[1])), mxu(mx4(a[2]), mx4(a[3]))) != 0xffffffffu)
    OLD();
    SCHED();
#pragma unroll
    for (int k = 0; k < 8; ++k) {
      const u32x2 lo = trd(lds, wa + k * 8192), hi = trd(lds, wa + k * 8192 + 512);
      bg[k] = u32x4{lo[0], lo[1], hi[0], hi[1]};
    }
#pragma unroll
    for (int k = 0; k < 8; ++k) asm volatile("" : "+v"(bg[k]));
    if (!OOK()) {
      int sp = 0;
      _Pragma("nounroll") do { nap(2); OLD(); } while (!OOK() && ++sp < SPIN_CAP);
    }
    STAMP(7); TSC(1);
#define OS(p, j) (__uint_as_float(a[p][j]) + __uint_as_float(a[(p) + 2][j]))
    *(u32x4*)(lds + iw) = u32x4{cvt2(OS(0, 0), OS(0, 1)), cvt2(OS(0, 2), OS(0, 3)), cvt2(OS(1, 0), OS(1, 1)), cvt2(OS(1, 2), OS(1, 3))};
    lds_barrier();
    TSC(2);
    u32x4 of[8];
#pragma unroll
    for (int k = 0; k < 8; ++k) { of[k] = *(const u32x4*)(lds + ir + k * 1024); SCHED(); }
    f32x4 acc = {0.f, 0.f, 0.f, 0.f};
#pragma unroll
    for (int k = 0; k < 8; ++k) acc = mfma(bg[k], of[k], acc);
    *(u32*)(lds + rw) = sanf(half_sum1(acc, n < 8));
    lds_barrier();
    TSC(3);
    if (lane < 16) st128(rs, xo, *(const u32x4*)(lds + rr));
    TSC(4);
    if (wave == 7) {
      u32x4 xp[2];
      _Pragma("nounroll") for (int sp = 0; sp < SPIN_CAP; ++sp) {
        xp[0] = ld128(rs, xi); xp[1] = ld128(rs, xi + 1024);
        if (__all(mxu(mx4(xp[0]), mx4(xp[1])) != 0xffffffffu)) break;
        nap(1);
      }
      TSC(5);
      const u32x2 r0 = gather4(rbf(xv[0] + rbf(ga[0] * rbf(red8(asf4(xp[0])))))), r1 = gather4(rbf(xv[1] + rbf(ga[1] * rbf(red8(asf4(xp[1]))))));
      st128(rs, xq, u32x4{sanu(r0[0]), sanu(r0[1]), sanu(r1[0]), sanu(r1[1])});
      if (lane == 0) st128(rs, W_TS + (oj * 8 + op) * 128, u32x4{(u32)__builtin_amdgcn_s_memrealtime(), 0u, e0, e1});
      STAMPW(8); TSC(6);
    }
  }
  if (oj >= 0) STAMP(13);
  if (gs >= 0) {
    constexpr int L_H2 = L_B;
    constexpr int L_ACT = L_C + 4608;
    const int b = wave;
    u32x4 bu[9], wdf[2][5];
    BAR(); SCHED();
    TSC(0);
    float sb[9], cb1[9];
    int ia[2], ra[4], yo[3], tw, tr_, xa, xb;
    u32x4 x8, fy = {e0, e1, e0, e1}; u32 x1;
    int fo = W_FY + min(lane, NPY / 2 - 1) * 16;
    {
#pragma unroll
      for (int j = 4; j < 9; ++j) dma(db + j * 8192, dv, lds_base + L_W + 73728 + (j * 8 + wave) * 1024);
      BAR();
      slab_dma<0, UA>(wu + gs * 64, FF * 2, lds_base + L_W, wave, lane);
#pragma unroll
    for (int i = 0; i < 2; ++i) {
      const int k = i == 0 ? lane * 8 : 512 + lane, hf = k >= 288 ? 1 : 0, kk = k - hf * 288, s = kk >> 5, g = (kk >> 3) & 3;
      ia[i] = L_H2 + (s * 64 + g * 16 + ((hf * 8 + b) ^ ((s & 3) * 4 + g))) * 16 + (kk & 7) * 2;
    }
#pragma unroll
    for (int i = 0; i < 4; ++i) ra[i] = L_H2 + (grp * 16 + (n ^ (i * 4 + grp))) * 16;
    tw = L_ACT + (n & 7) * 144 + (wave >> 1) * 32 + (grp & 1) * 16 + (wave & 1) * 8 + (grp >> 1) * 4 + (n >> 3) * 2; tr_ = L_ACT + ((n >> 2) * 2 + (n & 1)) * 144 + grp * 16;
    yo[0] = W_Y + (((grp * 2) * 18 + wave * 2 + (n >> 3)) * NG + gs) * 128 + (n & 7) * 16;
    yo[1] = W_Y + ((grp * 2 * 18 + 16 + ((wave >> 1) & 1)) * NG + gs) * 128 + (wave & 1) * 64 + n * 4;
    yo[2] = yo[0] + 18 * NG * 128;
    xa = W_X1 + (xcd * 8 + b) * 1152 + lane * 16; xb = W_X1 + (xcd * 8 + b) * 1152 + 1024 + (lane >> 1) * 4;
    asm volatile("" : "+v"(ia[0]), "+v"(ia[1]), "+v"(ra[0]), "+v"(ra[1]), "+v"(ra[2]), "+v"(ra[3]), "+v"(tw), "+v"(tr_), "+v"(yo[0]), "+v"(yo[1]), "+v"(yo[2]), "+v"(xa), "+v"(xb), "+v"(fo));
#define MOK() __all(mxu(mxu(mx4(mg[0]), mx4(mg[1])), mxu(mh[0], mh[1])) != 0xffffffffu)
#define MLD(b_) do { _Pragma("unroll") for (int i = 0; i < 2; ++i) { mg[i] = ld128(rs, W_M + (8 + i * 8 + (b_)) * 1152 + lane * 16); mh[i] = ld32(rs, W_M + (8 + i * 8 + (b_)) * 1152 + 1024 + (lane >> 1) * 4); } } while (0)
#define MUNP() do { _Pragma("unroll") for (int i = 0; i < 4; ++i) { sb[2 * i] = lo16(mg[0][i]); sb[2 * i + 1] = hi16(mg[0][i]); \
                      cb1[2 * i] = rbf(1.f + lo16(mg[1][i])); cb1[2 * i + 1] = rbf(1.f + hi16(mg[1][i])); } \
                    sb[8] = pick2(mh[0], lane); cb1[8] = rbf(1.f + pick2(mh[1], lane)); \
                    _Pragma("unroll") for (int i = 0; i < 9; ++i) asm volatile("" : "+v"(sb[i]), "+v"(cb1[i])); } while (0)
      if (!MOK()) {
        int sp = 0;
        _Pragma("nounroll") do { nap(2); MLD(b); } while (!MOK() && ++sp < SPIN_CAP);
      }
      MUNP();
      WAITV(UA); BAR();
      {
        const int q = n >> 2, p = lane & 3, r = 16 * (grp >> 1) + 4 * (grp & 1) + q;
        const int a0 = L_W + 73728 + r * 1152 + (p & 1) * 8;
#pragma unroll
        for (int t = 0; t < 5; ++t) {
          const int a = t < 4 ? a0 + wave * 128 + (((t + (p >> 1) * 4) ^ (((r + wave) >> 1) & 3)) << 4) : a0 + 1024 + ((((wave & 3) * 2 + (p >> 1)) ^ ((r >> 1) & 3)) << 4);
#pragma unroll
          for (int ks = 0; ks < 2; ++ks) {
            const u32x2 lo = trd(lds, a + ks * 36864), hi = trd(lds, a + ks * 36864 + 9216);
            wdf[ks][t] = u32x4{lo[0], lo[1], hi[0], hi[1]};
          }
        }
#pragma unroll
        for (int t = 0; t < 5; ++t) asm volatile("" : "+v"(wdf[0][t]), "+v"(wdf[1][t]));
      }
      TSC(1);
      slab_dma<UA, 9>(wu + gs * 64, FF * 2, lds_base + L_W, wave, lane);
      lds_barrier();
      slab_dma(wg + gs * 64, FF * 2, lds_base + L_W + 73728, wave, lane);
      asm volatile("s_waitcnt vmcnt(9)\n\ts_barrier" ::: "memory");
      slab_frags(lds, L_W, wave, lane, bu);
#pragma unroll
      for (int s = 0; s < 9; ++s) asm volatile("" : "+v"(bu[s]));
      TSC(2);
      asm volatile("s_waitcnt vmcnt(0)\n\ts_barrier" ::: "memory");
      x8 = ld128(rs, xa); x1 = ld32(rs, xb);
      slab_frags(lds, L_W + 73728, wave, lane, bg);
      STAMP(13);
#pragma unroll
      for (int s = 0; s < 9; ++s) asm volatile("" : "+v"(bg[s]));
    }
    {
      TSC(3);
#define XOK() __all(mxu(mx4(x8), x1) != 0xffffffffu)
      if (!XOK()) {
        int sp = 0;
        _Pragma("nounroll") do { nap(1); x8 = ld128(rs, xa); x1 = ld32(rs, xb); } while (!XOK() && ++sp < SPIN_CAP);
      }
      STAMP(9); TSC(4);
      if (wave == 0) fy = ld128(rs, fo);
      SCHED();
      u32x4 h8; u32 h1;
      ln9(x8, pick2(x1, lane), cb1, sb, h8, h1);
      *(u32x4*)(lds + ia[0]) = h8;
      *(u16*)(lds + ia[1]) = (u16)h1;
      TSC(5);
      lds_barrier();
      u32x4 af[9];
#pragma unroll
      for (int s = 0; s < 9; ++s) { af[s] = *(const u32x4*)(lds + ra[s & 3] + s * 1024); SCHED(); }
      f32x4 ag = {0.f, 0.f, 0.f, 0.f}, au = {0.f, 0.f, 0.f, 0.f};
#pragma unroll
      for (int s = 0; s < 9; ++s) ag = mfma(bg[s], af[s], ag);
      SCHED();
#pragma unroll
      for (int s = 0; s < 9; ++s) au = mfma(bu[s], af[s], au);
      const float g = rbf(silu(rbf(half_sum1(ag, n < 8))));
#pragma unroll
      for (int s = 0; s < 9; ++s) { __builtin_amdgcn_sched_group_barrier(0x8, 1, 0); __builtin_amdgcn_sched_group_barrier(0x2, 3, 0); }
      SCHED();
      *(u16*)(lds + tw) = (u16)cvt2(g * rbf(half_sum1(au, n < 8)), 0.f);
      SCHED();
      if (wave == 0 && !FOK(fy)) {
        int sp = 0;
        _Pragma("nounroll") do { nap(2); fy = ld128(rs, fo); } while (!FOK(fy) && ++sp < SPIN_CAP);
      }
    }
    lds_barrier();
    STAMP(10); TSC(6);
    {
      u32x4 af[2];
#pragma unroll
      for (int ks = 0; ks < 2; ++ks) af[ks] = *(const u32x4*)(lds + tr_ + ks * 64);
      f32x4 d[5];
#pragma unroll
      for (int t = 0; t < 4; ++t) d[t] = mfma(af[1], wdf[1][t], mfma(af[0], wdf[0][t], f32x4{0.f, 0.f, 0.f, 0.f}));
#pragma unroll
      for (int j = 0; j < 2; ++j)
        st128(rs, yo[2 * j], u32x4{sanf(d[0][j]), sanf(d[1][j]), sanf(d[2][j]), sanf(d[3][j])});
      SCHED();
      if (wave < 4) {
        d[4] = mfma(af[1], wdf[1][4], mfma(af[0], wdf[0][4], f32x4{0.f, 0.f, 0.f, 0.f}));
        __builtin_amdgcn_raw_buffer_store_b32(sanf(d[4][0]), rs, yo[1], 0, 16);
        __builtin_amdgcn_raw_buffer_store_b32(sanf(d[4][1]), rs, yo[1], 18 * NG * 128, 16);
      }
      TSC(7);
    }
  }
  {
  int t3_ = threadIdx.x; asm volatile("" : "+v"(t3_));
  const int tid = t3_, lane = tid & 63, n = lane & 15, grp = lane >> 4;
  const u32 t6_ = (u32)__builtin_amdgcn_s_memrealtime();
  if (idx >= 1 && idx < 14 && wave >= 2) {
    const int r_ = min(lane / 9, H2 - 1), c_ = lane - (lane / 9) * 9;
    ha = HROW(112 * H1 + ((wave - 2) * 13 + idx - 1) * H2 + r_) + c_ * 128;
    asm volatile("" : "+v"(ha));
    UNTIL(t6_ + HD2 + (wave - 2) * HS2);
    TSC(0);
    dma_keep64(ha, lds_base + L_B + 4096 + wave * 256);
    TSC(1);
    if (H3 > 0) {
      ha = HROW(112 * H1 + 78 * H2 + ((wave - 2) * 13 + idx - 1) * H3 + min(lane / 9, H3 - 1)) + c_ * 128;
      asm volatile("" : "+v"(ha));
      UNTIL(t6_ + HD3 + (wave - 2) * HS2);
      dma_keep64(ha, lds_base + L_B + 4096 + wave * 256);
    }
  }
  if ((qs >= 0 || mw >= 6) && wave >= 2) {
    const int py = qs >= 0 ? qs : 58 + mw;
    UNTIL(t6_ + YD);
#pragma unroll
    for (int i = 0; i < 3; ++i) {
      const int k_ = i * 384 + tid - 128, g_ = py * PY + k_;
      if (k_ < PY && g_ < 144 * NG * 8) st128(rs, W_Y + g_ * 16, u32x4{0xffffffffu, 0xffffffffu, 0xffffffffu, 0xffffffffu});
    }
    WAITV(0);
    if (lane == 0 && __hip_atomic_fetch_add((__attribute__((address_space(3))) u32*)((lds_t*)lds + L_B + 512), 1u, __ATOMIC_RELAXED, __HIP_MEMORY_SCOPE_WORKGROUP) == 5u)
      st64(rs, W_FY + py * 8, u32x2{e0, e1});
  }
  if (ml >= 0) {
    BAR();
    slab_dma(w_mod + ml * 64, NMOD * 2, lds_base + L_W, wave, lane);
    u32x4 wf[9], af[9];
    img_frags(lds, L_IMG, lane, af);
    asm volatile("s_waitcnt vmcnt(0)\n\ts_barrier" ::: "memory");
    slab_frags(lds, L_W, wave, lane, wf);
    const f32x4 s = half_sum(gemv9(wf, af));
    if (n < 8 && grp < 2) *(u32x2*)(lds + L_TILE + n * 128 + wave * 16 + grp * 8) = u32x2{cvt2(s[0], s[1]), cvt2(s[2], s[3])};
    lds_barrier();
    MPUB(ml);
  }

  if (idx >= 5 && idx < 23 && wave == 1) {
    const int b = xcd, jh = idx - 5, col = jh * 32 + (jh < 16 ? (lane & 7) + grp * 8 : (lane & 7) * 4 + grp);
    const rsrc_t rw_ = __builtin_amdgcn_make_buffer_rsrc((void*)w_r, 0, D * 2, 0x00020000);
    const u32x4 wr8 = __builtin_amdgcn_raw_buffer_load_b128(rw_, lane * 16, 0, 0);
    const float wr1 = b2f(__builtin_amdgcn_raw_buffer_load_b16(rw_, 1024 + lane * 2, 0, 0));
    u32x4 mg[2]; u32 mh[2];
    _Pragma("nounroll") for (int sp = 0; sp < SPIN_CAP; ++sp) { MLD(b); if (MOK()) break; nap(4); }
    float sb[9], cb1[9];
    MUNP();
    const int xa = W_X1 + (xcd * 8 + b) * 1152 + lane * 16, xb = W_X1 + (xcd * 8 + b) * 1152 + 1024 + (lane >> 1) * 4;
    u32x4 x8; u32 x1;
    _Pragma("nounroll") for (int sp = 0; sp < SPIN_CAP; ++sp) { x8 = ld128(rs, xa); x1 = ld32(rs, xb); if (XOK()) break; nap(1); }
    const u32 tx_ = (u32)__builtin_amdgcn_s_memrealtime();
    const int go = W_M + (24 + b) * 1152 + (col >> 1) * 4, fo = W_FY + min(lane, NPY / 2 - 1) * 16;
    u32 gq = ld32(rs, go); u32x4 fy = ld128(rs, fo);
    const float v8 = pick2(x1, lane);
    u32x4 h8; u32 h1;
    ln9(x8, v8, cb1, sb, h8, h1);
    unsigned char* row = lds + L_C;
    *(u32x4*)(row + lane * 16) = x8; *(u32x4*)(row + 1152 + lane * 16) = h8;
    *(u16*)(row + 1024 + lane * 2) = (u16)(__float_as_uint(v8) >> 16); *(u16*)(row + 2176 + lane * 2) = (u16)h1;
    float r = lo16(h1) * wr1;
#pragma unroll
    for (int i = 0; i < 4; ++i) r = fmaf(hi16(h8[i]), hi16(wr8[i]), fmaf(lo16(h8[i]), lo16(wr8[i]), r));
    r = rbf(wsum(r));
    float rsel = rbf(1.f / (1.f + __expf(-r)));
    float x1v = b2f(*(const u16*)(row + col * 2)), h2 = b2f(*(const u16*)(row + 1152 + col * 2));
    _Pragma("nounroll") for (int sp = 0; sp < SPIN_CAP && !(__all(gq != 0xffffffffu) && FOK(fy)); ++sp) { nap(2); gq = ld32(rs, go); fy = ld128(rs, fo); }
    float gbv = pick2(gq, col);
    u32 tgo;
    {
      const u32x4 tq = ld128(rs, W_TS + lane * 128);
      int d_ = tag_ok(tq, e0, e1) ? (int)(tq[0] - tx_) : 0;
      d_ = max(d_, (int)dppu<0xB1>(d_)); d_ = max(d_, (int)dppu<0x4E>(d_)); d_ = max(d_, (int)dppu<0x141>(d_)); d_ = max(d_, (int)dppu<0x140>(d_));
      d_ = max(max(__builtin_amdgcn_readlane(d_, 0), __builtin_amdgcn_readlane(d_, 16)), max(__builtin_amdgcn_readlane(d_, 32), __builtin_amdgcn_readlane(d_, 48)));
      tgo = tx_ + min(d_ + TGO, TGO - 15);
    }
    int yi = W_Y + (b * 18 + jh) * NG * 128 + lane * 16, oo = (b * D + col) * 2;
    asm volatile("" : "+v"(rsel), "+v"(x1v), "+v"(h2), "+v"(gbv), "+v"(yi), "+v"(oo));
    TSC(5);
    UNTIL(tgo);
    float z;
    _Pragma("nounroll") for (int sp = 0; sp < SPIN_CAP; ++sp) {
      u32x4 y[9];
#pragma unroll
      for (int i = 0; i < 9; ++i) y[i] = ld128(rs, yi + i * 1024);
      z = red8((((asf4(y[0]) + asf4(y[1])) + (asf4(y[2]) + asf4(y[3]))) + ((asf4(y[4]) + asf4(y[5])) + (asf4(y[6]) + asf4(y[7])))) + asf4(y[8]));
      if (__all(z == z)) break;
      u32 m = mx4(y[8]);
#pragma unroll
      for (int i = 0; i < 8; ++i) m = mxu(m, mx4(y[i]));
      if (__all(m != 0xffffffffu)) break;
      nap(1);
    }
    STAMPW(11); TSC(6);
    const float y2 = rbf(rbf(z) * rsel);
    const float res = x1v + rbf(gbv * rbf(h2 + y2));
    if (!(lane & 8)) __builtin_amdgcn_raw_buffer_store_b16((u16)(cvt2(res, 0.f) & 0xffffu), __builtin_amdgcn_make_buffer_rsrc(out, 0, 8 * D * 2, 0x00020000), oo, 0, 0);
    STAMPW(12); TSC(7);
  }
  TSOUT();
  }
  }
}

extern "C" void kernel_launch(void* const* d_in, const int* in_sizes, int n_in, void* d_out, int out_size, void* d_ws, size_t ws_size,
                              hipStream_t stream) {
  fused_kernel<<<NWG, NT, 0, stream>>>((const u16*)d_in[0], (const u16*)d_in[1], (const u16*)d_in[2], (const u16*)d_in[3], (const u16*)d_in[4],
                                       (const u16*)d_in[6], (const u16*)d_in[7], (const u16*)d_in[8], (const u16*)d_in[9], (const u16*)d_in[10],
                                       (const u16*)d_in[11], (unsigned char*)d_ws, (u16*)d_out);
}
```

```cpp
#include <hip/hip_runtime.h>
#include <hip/hip_bf16.h>
#include <cstdint>

typedef unsigned u32;
typedef unsigned short u16;
typedef u32 u32x4 __attribute__((ext_vector_type(4)));
typedef u32 u32x2 __attribute__((ext_vector_type(2)));
typedef float f32x4 __attribute__((ext_vector_type(4)));
typedef float f32x2 __attribute__((ext_vector_type(2)));
typedef short s16x4 __attribute__((ext_vector_type(4)));
typedef __bf16 bf16x8 __attribute__((ext_vector_type(8)));
typedef __bf16 bf16x16 __attribute__((ext_vector_type(16)));
typedef u32 u32x8 __attribute__((ext_vector_type(8)));
typedef __bf16 bf16x2 __attribute__((ext_vector_type(2)));
typedef __attribute__((address_space(3))) unsigned char lds_t;

constexpr int D = 576, SK = 8192, WIN = 2048, FF = 4608, NQ = 4096, NMOD = 6 * D;
constexpr int NWG = 256, NT = 512, NG = 72;
constexpr int SPIN_CAP = 1 << 15;

constexpr int W_O = 0;
constexpr int W_XP = W_O + 262144;
constexpr int W_X1 = W_XP + 147456;
constexpr int W_M = W_X1 + 73728;
constexpr int W_MS = W_M + 36864;
constexpr int W_Y = W_MS;
constexpr int W_FL = W_Y + 144 * NG * 128;
constexpr int W_FY = W_FL + 1024;
constexpr int W_H = W_FY + 1024;
constexpr int W_Q = W_H + 36864;
constexpr int W_ST = W_Q + 131072;
constexpr int W_TS = W_ST + 32768;
constexpr int W_END = W_TS + 72 * 128;
constexpr int PZ = (W_MS / 16 + NG - 1) / NG;
constexpr int NPY = 104;
static_assert(PZ <= NT && NPY * 13 * 1024 >= 144 * NG * 128, "poison split");

constexpr int L_W = 0;
constexpr int L_IMG = 73728;
constexpr int L_TILE = L_IMG + 9216;
constexpr int L_OI = 65536;
constexpr int L_B = 147456;
constexpr int L_C = 155648;
constexpr int LDS_BYTES = 163840;

#define STAMP(i) do {} while (0)
#define STAMPW(i) do {} while (0)

#define SCHED() __builtin_amdgcn_sched_barrier(0)
#define UNTIL(t) do { _Pragma("nounroll") while ((int)((u32)__builtin_amdgcn_s_memrealtime() - (t)) < 0) __builtin_amdgcn_s_sleep(1); asm volatile("" ::: "memory"); } while (0)
#define BAR() asm volatile("s_barrier" ::: "memory")
#define TS(i) do {} while (0)
#define TSOUT() do {} while (0)
#define TSA(i) do {} while (0)
#define TSB(i) do {} while (0)
#define TSC(i) do {} while (0)
#define WAITV_(n) do { asm volatile("s_waitcnt vmcnt(" #n ")" ::: "memory"); SCHED(); } while (0)
#define WAITV(n) WAITV_(n)
#define DK 5
#define DV DK
#define PR 117
#define LF 60
#define TX 160
#define H3 4
#define UA 5
#define NR 13
#define OD 40
#define OMASK 0
#define NB 4
#define HD3 70
#define H1 2
#define H2 5
#define HD2 15
#define HS2 0
#define TADJ -10
#define SLOWJ 6
#define TGO 190

__device__ __forceinline__ float lo16(u32 u) { return __uint_as_float(u << 16); }
__device__ __forceinline__ float hi16(u32 u) { return __uint_as_float(u & 0xffff0000u); }
__device__ __forceinline__ float b2f(u16 u) { return __uint_as_float((u32)u << 16); }
__device__ __forceinline__ u32 cvt2(float a, float b) { return __builtin_bit_cast(u32, __builtin_convertvector(f32x2{a, b}, bf16x2)); }
__device__ __forceinline__ float rbf(float v) { return __uint_as_float(cvt2(0.f, v)); }
__device__ __forceinline__ u32 sanf(float f) { u32 v = __float_as_uint(f); return v < 0xfffffffeu ? v : 0xfffffffeu; }
__device__ __forceinline__ u32 sanu(u32 v) { return v < 0xfffffffeu ? v : 0xfffffffeu; }
__device__ __forceinline__ f32x4 asf4(u32x4 v) { return __builtin_bit_cast(f32x4, v); }
__device__ __forceinline__ f32x2 up2(u32 u) { return f32x2{lo16(u), hi16(u)}; }
__device__ __forceinline__ f32x2 rbf2(f32x2 v) { return f32x2{rbf(v.x), rbf(v.y)}; }
__device__ __forceinline__ void lds_barrier() { asm volatile("s_waitcnt lgkmcnt(0)\n\ts_barrier" ::: "memory"); }
__device__ __forceinline__ f32x4 mfma(u32x4 a, u32x4 b, f32x4 c) {
  return __builtin_amdgcn_mfma_f32_16x16x32_bf16(__builtin_bit_cast(bf16x8, a), __builtin_bit_cast(bf16x8, b), c, 0, 0, 0);
}
__device__ __forceinline__ f32x4 smf(u32x4 a, u32x8 b, f32x4 c, int ix) {
  return __builtin_amdgcn_smfmac_f32_16x16x64_bf16(__builtin_bit_cast(bf16x8, a), __builtin_bit_cast(bf16x16, b), c, ix, 0, 0);
}
__device__ __forceinline__ f32x2 smf_sum(f32x4 d) { return f32x2{d[0], d[1]} + f32x2{d[2], d[3]}; }
__device__ __forceinline__ u32 plo(u32 a, u32 b) { return __builtin_amdgcn_perm(b, a, 0x05040100u); }
__device__ __forceinline__ u32 phi(u32 a, u32 b) { return __builtin_amdgcn_perm(b, a, 0x07060302u); }

extern "C" __device__ __attribute__((const)) unsigned long long llvm_dispatch_id() __asm("llvm.amdgcn.dispatch.id");
typedef __amdgpu_buffer_rsrc_t rsrc_t;
__device__ __forceinline__ u32x4 ld128(rsrc_t rs, int off) { return __builtin_amdgcn_raw_buffer_load_b128(rs, off, 0, 16); }
__device__ __forceinline__ u32 ld32(rsrc_t rs, int off) { return __builtin_amdgcn_raw_buffer_load_b32(rs, off, 0, 16); }
__device__ __forceinline__ void st128(rsrc_t rs, int off, u32x4 v) { __builtin_amdgcn_raw_buffer_store_b128(v, rs, off, 0, 16); }
__device__ __forceinline__ void st64(rsrc_t rs, int off, u32x2 v) { __builtin_amdgcn_raw_buffer_store_b64(v, rs, off, 0, 16); }
__device__ __forceinline__ void dma(const void* gbase, u32 voff, unsigned lds_dst) {
  unsigned keep;
  asm volatile("s_mov_b32 %0, m0\n\ts_mov_b32 m0, %3\n\ts_nop 2\n\tglobal_load_lds_dwordx4 %1, %2 nt\n\ts_mov_b32 m0, %0"
               : "=&s"(keep) : "v"(voff), "s"(gbase), "s"(lds_dst) : "memory");
}
__device__ __forceinline__ void dma_keep(const void* gbase, u32 voff, unsigned lds_dst) {
  unsigned keep;
  asm volatile("s_mov_b32 %0, m0\n\ts_mov_b32 m0, %3\n\ts_nop 2\n\tglobal_load_lds_dword %1, %2\n\ts_mov_b32 m0, %0"
               : "=&s"(keep) : "v"(voff), "s"(gbase), "s"(lds_dst) : "memory");
}
__device__ __forceinline__ void dma_keep64(const void* addr, unsigned lds_dst) {
  unsigned keep;
  asm volatile("s_mov_b32 %0, m0\n\ts_mov_b32 m0, %2\n\ts_nop 2\n\tglobal_load_lds_dword %1, off\n\ts_mov_b32 m0, %0"
               : "=&s"(keep) : "v"(addr), "s"(lds_dst) : "memory");
}
__device__ __forceinline__ void dma_sc(const void* gbase, u32 voff, unsigned lds_dst) {
  unsigned keep;
  asm volatile("s_mov_b32 %0, m0\n\ts_mov_b32 m0, %3\n\ts_nop 2\n\tglobal_load_lds_dwordx4 %1, %2 sc1\n\ts_mov_b32 m0, %0"
               : "=&s"(keep) : "v"(voff), "s"(gbase), "s"(lds_dst) : "memory");
}
__device__ __forceinline__ void ald128(u32x4& d, const void* gbase, u32 voff) { unsigned long long t; asm volatile("s_mov_b64 %1, %3\n\tglobal_load_dwordx4 %0, %2, %1" : "=v"(d), "=&s"(t) : "v"(voff), "s"(gbase) : "memory"); }
__device__ __forceinline__ void ald64(u32x2& d, const void* gbase, u32 voff) { unsigned long long t; asm volatile("s_mov_b64 %1, %3\n\tglobal_load_dwordx2 %0, %2, %1" : "=v"(d), "=&s"(t) : "v"(voff), "s"(gbase) : "memory"); }
__device__ __forceinline__ void ald128sc(u32x4& d, const void* gbase, u32 voff) { unsigned long long t; asm volatile("s_mov_b64 %1, %3\n\tglobal_load_dwordx4 %0, %2, %1 sc1" : "=v"(d), "=&s"(t) : "v"(voff), "s"(gbase) : "memory"); }
__device__ __forceinline__ u32x2 trd(const unsigned char* lds, int off) {
  return __builtin_bit_cast(u32x2, __builtin_amdgcn_ds_read_tr16_b64_v4i16((__attribute__((address_space(3))) s16x4*)((lds_t*)lds + off)));
}
#define nap(n) do { __builtin_amdgcn_s_sleep(n); asm volatile("" ::: "memory"); } while (0)
__device__ __forceinline__ float frcp(float v) { return __builtin_amdgcn_rcpf(v); }
__device__ __forceinline__ float fdiv(float a, float d, float r) { const float q = a * r; return fmaf(fmaf(-q, d, a), r, q); }
__device__ __forceinline__ float silu(float v) { const float d = 1.f + __expf(-v); const float r = frcp(d); return d < 3.0e38f ? fdiv(v, d, r) : v * r; }
template <int CTRL> __device__ __forceinline__ float dppf(float x) {
  return __builtin_bit_cast(float, __builtin_amdgcn_mov_dpp(__builtin_bit_cast(int, x), CTRL, 0xf, 0xf, true));
}
template <int CTRL> __device__ __forceinline__ u32 dppu(u32 x) { return (u32)__builtin_amdgcn_mov_dpp((int)x, CTRL, 0xf, 0xf, true); }
__device__ __forceinline__ u32 mxu(u32 a, u32 b) { return a > b ? a : b; }
__device__ __forceinline__ u32 mx4(u32x4 v) { return mxu(mxu(v[0], v[1]), mxu(v[2], v[3])); }
__device__ __forceinline__ float rl(float v, int l) { return __builtin_bit_cast(float, __builtin_amdgcn_readlane(__builtin_bit_cast(int, v), l)); }
__device__ __forceinline__ float wsum(float v) {
  v += dppf<0xB1>(v); v += dppf<0x4E>(v); v += dppf<0x141>(v); v += dppf<0x140>(v);
  return (rl(v, 0) + rl(v, 16)) + (rl(v, 32) + rl(v, 48));
}
__device__ __forceinline__ float up32(float v) { const u32 u = __float_as_uint(v); return __uint_as_float(__builtin_amdgcn_permlane32_swap(u, u, false, false)[1]); }
__device__ __forceinline__ float xsum32(float v) { const u32 u = __float_as_uint(v); const auto r = __builtin_amdgcn_permlane32_swap(u, u, false, false); return __uint_as_float(r[0]) + __uint_as_float(r[1]); }
__device__ __forceinline__ float xsum16(float v) { const u32 u = __float_as_uint(v); const auto r = __builtin_amdgcn_permlane16_swap(u, u, false, false); return __uint_as_float(r[0]) + __uint_as_float(r[1]); }
__device__ __forceinline__ float xmax32(float v) { const u32 u = __float_as_uint(v); const auto r = __builtin_amdgcn_permlane32_swap(u, u, false, false); return fmaxf(__uint_as_float(r[0]), __uint_as_float(r[1])); }
__device__ __forceinline__ float xmax16(float v) { const u32 u = __float_as_uint(v); const auto r = __builtin_amdgcn_permlane16_swap(u, u, false, false); return fmaxf(__uint_as_float(r[0]), __uint_as_float(r[1])); }
__device__ __forceinline__ float red8(f32x4 v) {
  const auto a = __builtin_amdgcn_permlane32_swap(__float_as_uint(v[0]), __float_as_uint(v[2]), false, false);
  const auto b = __builtin_amdgcn_permlane32_swap(__float_as_uint(v[1]), __float_as_uint(v[3]), false, false);
  const float w0 = __uint_as_float(a[0]) + __uint_as_float(a[1]), w1 = __uint_as_float(b[0]) + __uint_as_float(b[1]);
  const auto c = __builtin_amdgcn_permlane16_swap(__float_as_uint(w0), __float_as_uint(w1), false, false);
  const float z = __uint_as_float(c[0]) + __uint_as_float(c[1]);
  return z + dppf<0x128>(z);
}
__device__ __forceinline__ u32x2 gather4(float z) {
  const u32 u = __float_as_uint(z);
  const auto a = __builtin_amdgcn_permlane16_swap(u, u, false, false);
  const u32 pk = phi(a[0], a[1]);
  const auto b = __builtin_amdgcn_permlane32_swap(pk, pk, false, false);
  return u32x2{b[0], b[1]};
}
__device__ __forceinline__ f32x4 half_sum(f32x4 d) {
  f32x4 r;
#pragma unroll
  for (int j = 0; j < 4; ++j) r[j] = d[j] + dppf<0x108>(up32(d[j]));
  return r;
}
__device__ __forceinline__ float half_sum1(f32x4 d, bool lo) {
  const auto a = __builtin_amdgcn_permlane32_swap(__float_as_uint(d[0]), __float_as_uint(d[2]), false, false);
  const auto b = __builtin_amdgcn_permlane32_swap(__float_as_uint(d[1]), __float_as_uint(d[3]), false, false);
  return __uint_as_float(lo ? a[0] : b[1]) + dppf<0x128>(__uint_as_float(lo ? b[0] : a[1]));
}
__device__ __forceinline__ void unpack8(u32x4 u, float* f) {
#pragma unroll
  for (int j = 0; j < 4; ++j) { f[2 * j] = lo16(u[j]); f[2 * j + 1] = hi16(u[j]); }
}
__device__ __forceinline__ void ln_row(const float (&v)[16], bool has2, float& mean, float& den) {
  float s = 0.f;
#pragma unroll
  for (int i = 0; i < 8; ++i) s += v[i];
  if (has2) {
#pragma unroll
    for (int i = 8; i < 16; ++i) s += v[i];
  }
  s = wsum(s);
  const float mu = s * (1.f / D);
  mean = rbf(mu);
  float q = 0.f;
#pragma unroll
  for (int i = 0; i < 8; ++i) { const float t = v[i] - mu; q = fmaf(t, t, q); }
  if (has2) {
#pragma unroll
    for (int i = 8; i < 16; ++i) { const float t = v[i] - mu; q = fmaf(t, t, q); }
  }
  q = wsum(q);
  const float var = rbf(q * (1.f / D));
  den = rbf(sqrtf(rbf(var + 1e-5f)));
}
__device__ __forceinline__ float pick2(u32 t, int e) { return (e & 1) ? hi16(t) : lo16(t); }
__device__ __forceinline__ void ln9(u32x4 x8, float v8, const float (&cb1)[9], const float (&sb)[9], u32x4& h8, u32& h1) {
  f32x2 v[4];
#pragma unroll
  for (int i = 0; i < 4; ++i) v[i] = up2(x8[i]);
  const f32x2 s2 = (v[0] + v[1]) + (v[2] + v[3]);
  const float mu = wsum((s2.x + s2.y) + v8) * (1.f / D);
  const f32x2 mu2 = {mu, mu};
  const float t8 = v8 - mu;
  f32x2 q = {t8 * t8, 0.f};
#pragma unroll
  for (int i = 0; i < 4; ++i) { const f32x2 t = v[i] - mu2; q = __builtin_elementwise_fma(t, t, q); }
  const float mean = rbf(mu);
  f32x2 d[4];
#pragma unroll
  for (int i = 0; i < 4; ++i) d[i] = rbf2(v[i] - f32x2{mean, mean});
  const float d8 = rbf(v8 - mean);
  const float var = rbf(wsum(q.x + q.y) * (1.f / D));
  const float rden = frcp(rbf(__builtin_amdgcn_sqrtf(rbf(var + 1e-5f))));
#pragma unroll
  for (int i = 0; i < 4; ++i) {
    const f32x2 t = rbf2(rbf2(d[i] * f32x2{rden, rden}) * f32x2{cb1[2 * i], cb1[2 * i + 1]}) + f32x2{sb[2 * i], sb[2 * i + 1]};
    h8[i] = cvt2(t.x, t.y);
  }
  h1 = cvt2(rbf(rbf(d8 * rden) * cb1[8]) + sb[8], 0.f);
}
__device__ __forceinline__ void ln9f(u32x4 x8, float v8, const float (&cb1)[9], const float (&sb)[9], u32x4& h8, u32& h1) {
  const float s0 = lo16(__builtin_amdgcn_readfirstlane(x8[0]));
  f32x2 d[4];
#pragma unroll
  for (int i = 0; i < 4; ++i) d[i] = up2(x8[i]) - f32x2{s0, s0};
  const float d8 = v8 - s0;
  const f32x2 s2 = (d[0] + d[1]) + (d[2] + d[3]);
  const f32x2 q2 = __builtin_elementwise_fma(d[0], d[0], d[1] * d[1]) + __builtin_elementwise_fma(d[2], d[2], d[3] * d[3]);
  const auto r = __builtin_amdgcn_permlane32_swap(__float_as_uint((s2.x + d8) + s2.y), __float_as_uint(fmaf(d8, d8, q2.x) + q2.y), false, false);
  float z = __uint_as_float(r[0]) + __uint_as_float(r[1]);
  z += dppf<0xB1>(z); z += dppf<0x4E>(z); z += dppf<0x141>(z); z += dppf<0x140>(z);
  f32x2 t[4];
#pragma unroll
  for (int i = 0; i < 4; ++i) t[i] = d[i] * f32x2{cb1[2 * i], cb1[2 * i + 1]};
  const float md = (rl(z, 0) + rl(z, 16)) * (1.f / D);
  const float var = rbf(fmaf(-md, md, (rl(z, 32) + rl(z, 48)) * (1.f / D)));
  const float mr = s0 - rbf(s0 + md);
  const float rden = frcp(rbf(__builtin_amdgcn_sqrtf(rbf(var + 1e-5f))));
#pragma unroll
  for (int i = 0; i < 4; ++i) {
    const f32x2 u = __builtin_elementwise_fma(__builtin_elementwise_fma(f32x2{mr, mr}, f32x2{cb1[2 * i], cb1[2 * i + 1]}, t[i]), f32x2{rden, rden}, f32x2{sb[2 * i], sb[2 * i + 1]});
    h8[i] = cvt2(u.x, u.y);
  }
  h1 = cvt2(fmaf((d8 + mr) * cb1[8], rden, sb[8]), 0.f);
}
template <int J0 = 0, int J1 = 9>
__device__ __forceinline__ void slab_dma(const u16* base, int stride_b, unsigned lds_dst, int wave, int lane) {
  const u32 v0 = (lane >> 3) * stride_b + (((lane & 7) ^ ((wave & 1) * 4 + (lane >> 4))) << 4), v1 = v0 ^ 32u;
  const u32 v4 = wave >= 4 ? v1 : v0;
  const char* gb = (const char*)base + (size_t)wave * 8 * stride_b;
#pragma unroll
  for (int j = J0; j < J1; ++j) dma(gb + (size_t)j * 64 * stride_b, j < 4 ? v0 : j == 4 ? v4 : v1, lds_dst + (j * 8 + wave) * 1024);
}
__device__ __forceinline__ void slab_frags(const unsigned char* lds, int base, int wave, int lane, u32x4 (&wf)[9]) {
  const int g = lane >> 4, q = (lane & 15) >> 2, p = lane & 3, hf = p >> 1;
  const int a0 = base + (hf * 288 + 8 * g + q) * 128 + ((wave ^ ((g & 1) * 4 + (q >> 1)) ^ (hf * 2)) << 4) + (p & 1) * 8, a1 = (a0 ^ 32) + 512;
#pragma unroll
  for (int s = 0; s < 9; ++s) {
    const u32x2 lo = trd(lds, a0 + s * 4096), hi = trd(lds, a1 + s * 4096);
    wf[s] = u32x4{lo[0], lo[1], hi[0], hi[1]};
  }
}
__device__ __forceinline__ void img_frags(const unsigned char* lds, int img, int lane, u32x4 (&af)[9]) {
#pragma unroll
  for (int s = 0; s < 9; ++s) af[s] = *(const u32x4*)(lds + img + (s * 64 + lane) * 16);
}
__device__ __forceinline__ f32x4 gemv9(const u32x4 (&wf)[9], const u32x4 (&af)[9]) {
  f32x4 acc[3];
  SCHED();
#pragma unroll
  for (int s = 0; s < 9; ++s) acc[s % 3] = mfma(wf[s], af[s], s < 3 ? f32x4{0.f, 0.f, 0.f, 0.f} : acc[s % 3]);
  return (acc[0] + acc[1]) + acc[2];
}
__device__ __forceinline__ int img_off(int b, int k8) {
  const int hf = k8 >= 36 ? 1 : 0, kk = k8 - hf * 36;
  return ((kk >> 2) * 64 + (kk & 3) * 16 + hf * 8 + b) * 16;
}
__device__ __forceinline__ u32 tag_x(u32x4 v, u32 e0, u32 e1) { return (v[2] ^ e0) | (v[3] ^ e1); }
__device__ __forceinline__ bool tag_ok(u32x4 v, u32 e0, u32 e1) { return tag_x(v, e0, e1) == 0u; }

constexpr int CODE_BYTES = 31232;

__global__ void __launch_bounds__(NT, 2) fused_kernel(const u16* __restrict__ x, const u16* __restrict__ kc, const u16* __restrict__ vc,
                                                      const u16* __restrict__ wq, const u16* __restrict__ wo, const u16* __restrict__ w_r,
                                                      const u16* __restrict__ wg, const u16* __restrict__ wu, const u16* __restrict__ wd,
                                                      const u16* __restrict__ c, const u16* __restrict__ w_mod, unsigned char* ws, u16* out) {
  __shared__ __attribute__((aligned(16))) unsigned char lds[LDS_BYTES];
  int w = blockIdx.x;
  const int tid = threadIdx.x, lane = tid & 63, wave = __builtin_amdgcn_readfirstlane(tid >> 6);
  const int n = lane & 15, grp = lane >> 4;
  const size_t pairbase = ((size_t)(w >> 1) * SK + (SK - WIN)) * 256 + (w & 1) * 16384;
  const int wpart = (wave & 3) * 4096 + (wave >> 2) * 262144;
  const int ko = wpart + (n >> 2) * 256 + grp * 64 + (n & 3) * 16;
  const char* kp = (const char*)kc + pairbase;
  u32x4 kb[32], vb[32];
#define KLD(u) do { unsigned long long t_; asm volatile("s_mov_b64 %1, %3\n\tglobal_load_dwordx4 %0, %2, %1 offset:%4 nt" : "=v"(kb[u]), "=&s"(t_) : "v"(ko), "s"(kp + ((u) >> 2) * 32768), "i"(((u) & 3) * 1024) : "memory"); } while (0)
  unsigned long long tst;
  asm volatile("s_memrealtime %0\n\tglobal_load_dwordx4 %1, %5, %6 nt\n\tglobal_load_dwordx4 %2, %5, %6 offset:1024 nt\n\t"
               "global_load_dwordx4 %3, %5, %6 offset:2048 nt\n\tglobal_load_dwordx4 %4, %5, %6 offset:3072 nt"
               : "=&s"(tst), "=&v"(kb[0]), "=&v"(kb[1]), "=&v"(kb[2]), "=&v"(kb[3]) : "v"(ko), "s"(kp) : "memory");
  asm volatile("s_waitcnt lgkmcnt(0)" : "+s"(w), "+s"(ws), "+s"(tst));
  const u32 tstart = (u32)tst;
  const int xcd = w & 7, idx = w >> 3;
  const rsrc_t rs = __builtin_amdgcn_make_buffer_rsrc(ws, 0, W_END, 0x00020000);
  const unsigned lds_base = (unsigned)(uintptr_t)(lds_t*)lds;
  const unsigned long long epoch = (llvm_dispatch_id() + 1ull) * 0x9E3779B97F4A7C15ull;
  const u32 e0 = (u32)epoch, e1 = (u32)(epoch >> 32);
  unsigned long long pc0; asm volatile("s_getpc_b64 %0" : "=s"(pc0));
  asm volatile("" : "+s"(vc), "+s"(w_r), "+s"(wu), "+s"(wd), "+s"(out), "+s"(x), "+s"(w_mod), "+s"(wg), "+s"(wq), "+s"(wo));

  const int mw = idx < 6 ? idx * 8 + xcd : -1;
  const int md = mw < 18 ? mw : -1;
  const int me = mw >= 18 ? mw : -1;
  const int ml = mw >= 0 && mw < 6 ? 48 + mw : -1;
  const int qs = idx >= 6 && idx < 14 ? xcd * 8 + idx - 6 : -1;
  const int oj = idx >= 14 && idx < 23 ? idx - 14 : -1, op = xcd;
  const int gs = idx >= 23 ? xcd * 9 + idx - 23 : -1;

  const int s_att = w & 1, kvh = (w >> 1) & 15, b_att = w >> 5;
  {
    const char* vp = (const char*)vc + pairbase;
    const int vo = wpart + lane * 16;
#define VLD(u) do { unsigned long long t_; asm volatile("s_mov_b64 %1, %3\n\tglobal_load_dwordx4 %0, %2, %1 offset:%4 nt" : "=v"(vb[u]), "=&s"(t_) : "v"(vo), "s"(vp + ((u) >> 2) * 32768), "i"(((u) & 3) * 1024) : "memory"); } while (0)
#define TIE8(a, o) asm volatile("" : "+v"(a[o]), "+v"(a[(o) + 1]), "+v"(a[(o) + 2]), "+v"(a[(o) + 3]), "+v"(a[(o) + 4]), "+v"(a[(o) + 5]), "+v"(a[(o) + 6]), "+v"(a[(o) + 7]))
    constexpr int pr_ = PR;
    const int tx_ = gs >= 0 ? TX - LF : TX;
    const u32 tp0 = tstart + tx_ - TADJ;
    const int ramp_ = gs >= 0 ? (TX - LF) * 16 / NR : oj >= 0 ? TX * 16 / NR : 0;
#define PACE(i) do { _Pragma("nounroll") while ((int)((u32)__builtin_amdgcn_s_memrealtime() - tp0) < (i) * pr_ / 4 - ((i) < NR ? ((NR - (i)) * ramp_) >> 4 : 0)) __builtin_amdgcn_s_sleep(2); SCHED(); } while (0)
#define REP4(M, u, W) do { M(u); W; M((u) + 1); W; M((u) + 2); W; M((u) + 3); W; } while (0)
    static_assert(DK <= 7 && DV <= 7, "the hand-placed waits assume it");
#define POISON() do { if (gs >= 0) { const int g_ = gs * PZ + tid; \
      if (tid < PZ && g_ < W_MS / 16) st128(rs, g_ * 16, u32x4{0xffffffffu, 0xffffffffu, 0xffffffffu, 0xffffffffu}); } SCHED(); } while (0)
#define PFLAG() do { if (gs >= 0 && tid == 448) st64(rs, W_FL + gs * 8, u32x2{e0, e1}); SCHED(); } while (0)
#define FOK(a) __all(((a[0] ^ e0) | (a[1] ^ e1) | tag_x(a, e0, e1)) == 0u)
#define FREQ() do { if (wave == 1) dma_sc(ws + W_FL, min(lane, NG / 2 - 1) * 16, lds_base + L_B + 2048); } while (0)
#define FCHK() do { if (wave == 1 && !FOK((*(const u32x4*)(lds + L_B + 2048 + lane * 16)))) { \
      _Pragma("nounroll") for (int sp = 0; sp < SPIN_CAP; ++sp) { nap(4); const u32x4 a_ = ld128(rs, W_FL + min(lane, NG / 2 - 1) * 16); if (FOK(a_)) break; } } SCHED(); } while (0)
#define SILU_IMAGE(c0, c1) do { float f[8]; unpack8(c0, f); u32x4 o; \
        _Pragma("unroll") for (int j = 0; j < 4; ++j) o[j] = cvt2(silu(f[2 * j]), silu(f[2 * j + 1])); \
        const int b_ = tid / 72, ch_ = tid - b_ * 72; \
        *(u32x4*)(lds + L_IMG + img_off(b_, ch_)) = o; \
        if (wave == 0) { unpack8(c1, f); \
          _Pragma("unroll") for (int j = 0; j < 4; ++j) o[j] = cvt2(silu(f[2 * j]), silu(f[2 * j + 1])); \
          *(u32x4*)(lds + L_IMG + img_off(7, 8 + lane)) = o; } } while (0)
    if (md >= 0) {
      u32x4 c0, c1, x0, x1; u32x2 xc;
      TSA(0);
      int ho = W_H + img_off(n & 7, (md >= 9 ? md - 9 : md) * 8 + wave) * 2 + (grp & 1) * 16;
      ald128(c0, c, tid * 16);
      ald128(c1, c, (512 + lane) * 16);
      ald128(x0, x, wave * 1152 + lane * 16);
      ald128(x1, x, wave * 1152 + 1024 + (lane & 7) * 16);
      ald64(xc, x, (n & 7) * 1152 + ((md >= 9 ? md - 9 : 0) * 64 + wave * 8 + (grp & 1) * 4) * 2);
      BAR();
      float* st = (float*)(lds + L_TILE + 1024);
      if (wave < 4) {
        slab_dma(w_mod + md * 64, NMOD * 2, lds_base + L_W, wave, lane);
        asm volatile("s_waitcnt vmcnt(9)" ::: "memory");
      } else asm volatile("s_waitcnt vmcnt(0)" ::: "memory");
      asm volatile("s_nop 0 ; both branches above cover V" : "+v"(c0), "+v"(c1), "+v"(x0), "+v"(x1), "+v"(xc) :: "memory");
      TSA(1);
      {
        SILU_IMAGE(c0, c1);
        float v[16]; unpack8(x0, v); unpack8(x1, v + 8);
        float mean, den; ln_row(v, lane < 8, mean, den);
        if (lane == 0) { st[wave] = mean; st[8 + wave] = den; }
      }
      TSA(2);
      if (wave >= 4) slab_dma(w_mod + md * 64, NMOD * 2, lds_base + L_W, wave, lane);
      TSA(3);
      lds_barrier();
      REP4(KLD, 4, );
      u32x4 wf[9], af[9];
      img_frags(lds, L_IMG, lane, af);
      const float mean = st[n & 7], rd = frcp(st[8 + (n & 7)]);
      float ln[4] = {rbf(rbf(lo16(xc[0]) - mean) * rd), rbf(rbf(hi16(xc[0]) - mean) * rd), rbf(rbf(lo16(xc[1]) - mean) * rd), rbf(rbf(hi16(xc[1]) - mean) * rd)};
#pragma unroll
      for (int s = 0; s < 9; ++s) asm volatile("" : "+v"(af[s]));
      asm volatile("" : "+v"(ln[0]), "+v"(ln[1]), "+v"(ln[2]), "+v"(ln[3]), "+v"(ho));
      WAITV(4); BAR(); SCHED();
      TSA(5);
      slab_frags(lds, L_W, wave, lane, wf);
      const f32x4 s = half_sum(gemv9(wf, af));
      STAMP(14); TSA(6);
      f32x4 r = s;
      if (md >= 9) {
#pragma unroll
        for (int j = 0; j < 4; ++j) r[j] = ln[j] * rbf(1.f + rbf(s[j]));
      }
      if (n < 8 && grp < 2) st128(rs, ho + (md >= 9 ? 18432 : 0), u32x4{cvt2(r[0], r[1]), cvt2(r[2], r[3]), e0, e1});
      SCHED();
      STAMP(15); TSA(7);
      BAR();
      KLD(8); KLD(9); WAITV(DK); KLD(10); WAITV(DK);
      KLD(11); WAITV(DK); REP4(KLD, 12, WAITV(DK)); REP4(KLD, 16, WAITV(DK));
      FREQ(); REP4(KLD, 20, WAITV(DK)); PACE(24); REP4(KLD, 24, WAITV(DK)); PACE(28); KLD(28); WAITV(DK); KLD(29);
      FCHK();
    } else {
      constexpr int L_LAND = 98304;
      const unsigned land = lds_base + L_LAND + wave * 6144;
      const unsigned char* lp = lds + L_LAND + wave * 6144 + lane * 16;
      const int sl_ = (3 - (int)((uintptr_t)wq >> 7)) & 7, sm_ = (63 - (int)((uintptr_t)wq >> 7)) & 63;
      const bool slowq = qs >= 0 && ((qs & 7) == sl_ || qs == sm_);
      if (oj >= 0 && wave < 2) {
        if (wave == 0 || (sm_ >> 3) == xcd) dma_keep((const char*)wq + (size_t)oj * 64 * 8192 + (wave == 0 ? xcd * 8 + sl_ : sm_) * 128, lane * 8192, land);
      }
      POISON();
      if (oj >= 0 && oj < 8 && wave == 4)
        dma_keep((const void*)pc0, min((oj & 3) * 8192 + lane * 128, CODE_BYTES - 128), land);
      const u32 v0 = (lane >> 3) * 1152 + (((lane & 7) ^ (lane >> 4)) << 4);
      const char* gb = (const char*)(wo + (size_t)(op * 512 + wave * 64) * D + oj * 64);
      if (me >= 0) { dma_sc(c, tid * 16, land); dma_sc(c, (512 + lane) * 16, land + 1024); }
      const bool fs = qs >= 0 || me >= 0;
      const u16* sbase = qs >= 0 ? wq + qs * 64 : w_mod + me * 64;
      const int sstr = qs >= 0 ? NQ * 2 : NMOD * 2;
      const unsigned sdst = lds_base + L_W;
#define WK(j) do { if (!fs) { PACE((j) + 4); \
                     if (oj >= 0 && ((j) & 1) && NB + (j) / 2 < 8) dma(gb + (NB + (j) / 2) * 8 * 1152, v0 ^ (((NB + (j) / 2) & 1) * 64), lds_base + L_W + (wave * 8 + NB + (j) / 2) * 1024); } \
                   else if (slowq) { if ((j) == SLOWJ) slab_dma(sbase, sstr, sdst, wave, lane); } \
                   else slab_dma<j, (j) + 1>(sbase, sstr, sdst, wave, lane); \
                   KLD((j) + 4); WAITV(DK); } while (0)
      WK(0); WK(1); WK(2); WK(3);
      WK(4); WK(5);
      static_assert(DK <= 5, "six younger requests cover the poison");
      if (me >= 0) { const u32x4 c0 = *(const u32x4*)lp, c1 = *(const u32x4*)(lp + 1024); SILU_IMAGE(c0, c1); }
      BAR();
      STAMP(2);
      PFLAG();
      WK(6); WK(7); WK(8);

      PACE(13); KLD(13); WAITV(DK); KLD(14); WAITV(DK); KLD(15); WAITV(DK);
      const int hg0 = (wave * 144 + lane) * 16, hg2 = (wave * 144 + 128 + n) * 16;
#define HREQ() do { dma_sc(ws + W_H, hg0, land); dma_sc(ws + W_H + 18432, hg0, land + 1024); dma_sc(ws + W_H + 1024, hg0, land + 2048); \
                    dma_sc(ws + W_H + 18432 + 1024, hg0, land + 3072); dma_sc(ws + W_H, hg2, land + 4096); dma_sc(ws + W_H + 18432, hg2, land + 5120); } while (0)
#define HGET() do { _Pragma("unroll") for (int i = 0; i < 6; ++i) hq[i] = *(const u32x4*)(lp + i * 1024); } while (0)
#define HQOK() __all((tag_x(hq[0], e0, e1) | tag_x(hq[1], e0, e1) | tag_x(hq[2], e0, e1) | tag_x(hq[3], e0, e1) | tag_x(hq[4], e0, e1) | tag_x(hq[5], e0, e1)) == 0u)
      PACE(16); KLD(16); WAITV(DK); KLD(17); WAITV(DK); PACE(18); KLD(18); WAITV(DK); KLD(19); WAITV(DK);
      TSA(4);
      if (qs >= 0) HREQ();
      PACE(20); FREQ(); KLD(20); WAITV(DK); KLD(21); WAITV(DK); PACE(22); KLD(22); WAITV(DK); KLD(23); WAITV(DK);
      if (qs >= 0) WAITV(4);
      TSA(5);
      PACE(24); KLD(24); KLD(25);
      if (qs >= 0) {
        u32x4 hq[6]; HGET();
        _Pragma("nounroll") for (int sp = 0; sp < SPIN_CAP && !HQOK(); ++sp) { nap(16); HREQ(); WAITV(0); HGET(); }
        STAMP(14); TSA(6);
#define HSUM(a, b) cvt2(lo16(a) + lo16(b), hi16(a) + hi16(b))
        *(u32x2*)(lds + L_IMG + hg0 / 2) = u32x2{HSUM(hq[0][0], hq[1][0]), HSUM(hq[0][1], hq[1][1])};
        *(u32x2*)(lds + L_IMG + 512 + hg0 / 2) = u32x2{HSUM(hq[2][0], hq[3][0]), HSUM(hq[2][1], hq[3][1])};
        if (lane < 16) *(u32x2*)(lds + L_IMG + hg2 / 2) = u32x2{HSUM(hq[4][0], hq[5][0]), HSUM(hq[4][1], hq[5][1])};
      } else WAITV(DK);
      KLD(26); KLD(27);
      if (fs) {
        u32x4 wf[9], af[9];
        lds_barrier();
        slab_frags(lds, L_W, wave, lane, wf);
        img_frags(lds, L_IMG, lane, af);
        const f32x4 s = half_sum(gemv9(wf, af));
        if (n < 8 && grp < 2) *(u32x2*)(lds + L_TILE + n * 128 + wave * 16 + grp * 8) = u32x2{cvt2(s[0], s[1]), cvt2(s[2], s[3])};
      } else WAITV(DK);
      PACE(28); KLD(28); KLD(29);
      WAITV(9); FCHK();
#define MPUB(m_) do { if (wave == 0) { const u32x4 t_ = *(const u32x4*)(lds + L_TILE + lane * 16); const int ch_ = ((m_) - 18) / 9, sl_2 = (m_) - 18 - ch_ * 9; \
          st128(rs, W_M + (ch_ * 8 + (lane >> 3)) * 1152 + sl_2 * 128 + (lane & 7) * 16, u32x4{sanu(t_[0]), sanu(t_[1]), sanu(t_[2]), sanu(t_[3])}); } } while (0)
      if (fs) {
        lds_barrier();
        if (qs < 0) MPUB(me);
        else if (wave < 2) {
          const u32x2 t2 = *(const u32x2*)(lds + L_TILE + tid * 8);
          st128(rs, W_Q + qs * 256 + (tid >> 4) * 16384 + n * 16, u32x4{t2[0], t2[1], e0, e1});
        }
        STAMP(15); TSA(7);
      }
      SCHED();
    }
    STAMP(3); TSB(0);
    const unsigned char* qg = ws + W_Q + (b_att * 1024 + kvh * 64) * 16;
    PACE(30); KLD(30); WAITV(DK); KLD(31); WAITV(DK);
    TSB(1);
    PACE(32); VLD(0); WAITV(DK); VLD(1); WAITV(DK); PACE(34); VLD(2); WAITV(DK); VLD(3); WAITV(DK);
    PACE(36); VLD(4); WAITV(DK); VLD(5); WAITV(DK);
    if (wave == 1) dma_sc(qg, lane * 16, lds_base + L_B + 1024);
    PACE(38); VLD(6); WAITV(DK); VLD(7); WAITV(DK);
    PACE(40); VLD(8); WAITV(DV); VLD(9); WAITV(DV);
    if (wave == 1) dma_sc(qg, lane * 16, lds_base + L_B + 1024);
    PACE(42); VLD(10); WAITV(DV); VLD(11); WAITV(DV);
    TIE8(kb, 0); TIE8(kb, 8); TIE8(kb, 16); TIE8(kb, 24);
    PACE(44); VLD(12); VLD(13);
    u32x4 qf[2];
    {
      const u32x4* ql = (const u32x4*)(lds + L_B + 1024 + (((n >> 2) & 1) * 32 + grp * 8 + (n & 3) * 2) * 16);
      u32x4 g[2];
      _Pragma("nounroll") for (int sp = 0; sp < SPIN_CAP; ++sp) {
        g[0] = ql[0]; g[1] = ql[1];
        if (__all((tag_x(g[0], e0, e1) | tag_x(g[1], e0, e1)) == 0u)) break;
        if (wave == 1) { dma_sc(qg, lane * 16, lds_base + L_B + 1024); WAITV(0); } else nap(4);
      }
      asm volatile("" ::: "memory");
      g[0] = ql[0]; g[1] = ql[1];
      qf[0] = n < 8 ? u32x4{g[0][0], g[0][1], g[1][0], g[1][1]} : u32x4{0, 0, 0, 0};
      qf[1] = n < 8 ? u32x4{0, 0, 0, 0} : u32x4{g[0][0], g[0][1], g[1][0], g[1][1]};
    }
    STAMP(4); TSB(2);
    constexpr float SCALE = 0.088388347648318440f;
    float sc[4];
#pragma unroll
    for (int i = 0; i < 4; ++i) {
      float y[2];
#pragma unroll
      for (int t = 0; t < 2; ++t) {
        const int u = 8 * t + 2 * i;
        const f32x4 a = mfma(kb[u + 16], qf[1], mfma(kb[u], qf[0], f32x4{0.f, 0.f, 0.f, 0.f}));
        const f32x4 b = mfma(kb[u + 17], qf[1], mfma(kb[u + 1], qf[0], f32x4{0.f, 0.f, 0.f, 0.f}));
        const float own = (n & 2) ? ((n & 1) ? b[3] : a[2]) : ((n & 1) ? b[1] : a[0]), oth = (n & 2) ? ((n & 1) ? a[3] : b[2]) : ((n & 1) ? a[1] : b[0]);
        y[t] = own + dppf<0xB1>(oth);
      }
      sc[i] = ((n & 2) ? y[1] : y[0]) + dppf<0x4E>((n & 2) ? y[0] : y[1]);
      if (i == 1 || i == 3) { SCHED(); if (i == 1) VLD(14); else VLD(15); SCHED(); }
    }
#pragma unroll
    for (int i = 0; i < 4; ++i) sc[i] = rbf(rbf(sc[i]) * SCALE);
    float mx = fmaxf(fmaxf(sc[0], sc[1]), fmaxf(sc[2], sc[3]));
    mx = fmaxf(mx, dppf<0xB1>(mx)); mx = fmaxf(mx, dppf<0x4E>(mx)); mx = fmaxf(mx, dppf<0x128>(mx));
    mx = xmax32(xmax16(mx));
#pragma unroll
    for (int i = 0; i < 4; ++i) sc[i] = __expf(sc[i] - mx);
    float ls = (sc[0] + sc[1]) + (sc[2] + sc[3]);
    ls += dppf<0xB1>(ls); ls += dppf<0x4E>(ls); ls += dppf<0x128>(ls);
    ls = xsum32(xsum16(ls));
    float* stat = (float*)(lds + L_B);
    if (tid == 0) *(u32*)(lds + L_B + 512) = 0u;
    if (lane == 0 || lane == 4) *(f32x2*)(stat + (wave * 2 + (lane >> 2)) * 2) = f32x2{mx, ls};
    SCHED(); PACE(48); VLD(16); VLD(17); SCHED();
    TSB(3);
    lds_barrier();
    TSB(4);
    float M = -INFINITY, L = 0.f;
    {
      f32x2 t[8];
#pragma unroll
      for (int q = 0; q < 8; ++q) { t[q] = *(const f32x2*)(stat + (q * 2 + ((n >> 2) & 1)) * 2); M = fmaxf(M, t[q].x); }
#pragma unroll
      for (int q = 0; q < 8; ++q) L = fmaf(__expf(t[q].x - M), t[q].y, L);
    }
    if (tid < 8) st128(rs, W_ST + (w * 8 + lane) * 16, u32x4{__float_as_uint(M), __float_as_uint(L), e0, e1});
    SCHED();
#define VTR(G) do { u32x4 f_[8]; _Pragma("unroll") for (int t = 0; t < 8; ++t) _Pragma("unroll") for (int j = 0; j < 4; ++j) \
      f_[t][j] = (t & 1) ? phi(vb[(G) * 8 + 2 * j][t >> 1], vb[(G) * 8 + 2 * j + 1][t >> 1]) : plo(vb[(G) * 8 + 2 * j][t >> 1], vb[(G) * 8 + 2 * j + 1][t >> 1]); \
      _Pragma("unroll") for (int t = 0; t < 8; ++t) { vb[(G) * 8 + t] = f_[t]; asm volatile("" : "+v"(vb[(G) * 8 + t])); } SCHED(); } while (0)
    WAITV(DV);
    TIE8(vb, 0);
    VTR(0);
    if (gs >= 0) {
      const unsigned char* mb = ws + W_M + (8 + wave) * 1152;
      const unsigned ld_ = lds_base + L_W + wave * 2304;
      dma_sc(mb, lane * 16, ld_); dma_sc(mb + 9216, lane * 16, ld_ + 1024);
      if (lane < 16) dma_sc(mb + 1024, (lane >> 3) * 9216 + (lane & 7) * 16, ld_ + 2048);
    } else if (oj >= 0 && tid >= 504) {
      dma_sc(ws + W_M + op * 1152 + oj * 128, (lane & 7) * 16, lds_base + L_B + 3072 - 896);
      dma_sc((const char*)x + op * 1152 + oj * 128, (lane & 7) * 16, lds_base + L_B + 3200 - 896);
    }
    PACE(50); VLD(18); WAITV(DV); VLD(19); WAITV(DV);
    PACE(52); VLD(20); WAITV(5);
    TIE8(vb, 8);
    VTR(1);
    VLD(21); WAITV(DV);
    const int po = W_ST + ((w ^ 1) * 8 + (n & 4)) * 16;
    PACE(54);
    VLD(22); WAITV(DV); VLD(23); WAITV(DV);
    u32x4 ps;
    ald128sc(ps, ws, po);
    PACE(56);
    VLD(24); WAITV(DV); VLD(25); WAITV(DV);
    PACE(58);
    VLD(26); WAITV(DV); VLD(27); WAITV(DV);
    VLD(28); VLD(29); WAITV(6);
    asm volatile("" : "+v"(ps));
    TIE8(vb, 16);
    VLD(30); VLD(31);
    VTR(2);
    if (!__all(tag_ok(ps, e0, e1))) {
      int sp = 0;
      _Pragma("nounroll") do { nap(1); ps = ld128(rs, po); } while (!__all(tag_ok(ps, e0, e1)) && ++sp < SPIN_CAP);
    }
    float fac;
    {
      const float M2 = __uint_as_float(ps[0]), L2 = __uint_as_float(ps[1]);
      const float Mg = fmaxf(M, M2);
      const float Lg = __expf(M - Mg) * L + __expf(M2 - Mg) * L2;
      fac = __expf(mx - Mg) / Lg;
    }
    STAMP(1); TSB(5);
    u32x4 pf[4];
#pragma unroll
    for (int j = 0; j < 4; ++j) {
      const float p_ = sc[j] * fac;
      pf[0][j] = cvt2(p_, dppf<0xB1>(p_));
      pf[1][j] = dppu<0x102>(pf[0][j]); pf[2][j] = dppu<0x108>(pf[0][j]); pf[3][j] = dppu<0x10A>(pf[0][j]);
    }
    f32x4 oa[8];
#pragma unroll
    for (int t = 0; t < 8; ++t) oa[t] = f32x4{0.f, 0.f, 0.f, 0.f};
    SCHED();
#pragma unroll
    for (int G = 0; G < 2; ++G)
#pragma unroll
      for (int t = 0; t < 8; ++t) oa[t] = mfma(pf[G], vb[G * 8 + t], oa[t]);
    SCHED();
#pragma unroll
    for (int t = 0; t < 8; ++t) oa[t] = mfma(pf[2], vb[16 + t], oa[t]);
    SCHED();
    int t2_ = threadIdx.x; asm volatile("" : "+v"(t2_));
    const int tid = t2_, lane = tid & 63, n = lane & 15, grp = lane >> 4;
    int oo = W_O + ((kvh * 8 + b_att) * 2 + s_att) * 1024 + lane * 16;
    int mw_ = L_C + wave * 1024 + lane * 32, mr_ = L_C + (grp * 16 + (lane & 7) * 2 + (n >> 3)) * 16;
    asm volatile("" : "+v"(oo), "+v"(mw_), "+v"(mr_), "+v"(pf[3]));
    u32x4 mg[2]; u32 mh[2];
    const u32 dv = (lane >> 3) * (gs >= 0 ? 128 : 1152) + (((lane & 7) ^ (lane >> 4)) << 4);
    const char* db = gs >= 0 ? (const char*)(wd + (size_t)gs * 64 * D) + wave * 1024 : (const char*)(wo + (size_t)(op * 512 + wave * 64) * D + oj * 64);
    constexpr int NU = 112 * H1 + 78 * (H2 + H3) - 576, NS = (2 * (576 - NU) + 111) / 112;
    static_assert(NU >= 0 && NU < 576 && H1 * 9 + NS <= 64 && H2 * 9 <= 64 && H3 * 9 <= 64, "helper split");
#define HROW(i_) (((i_) < NU ? (const char*)wu + (size_t)(576 - NU) * 9216 : (const char*)wg - (size_t)NU * 9216) + (size_t)(i_) * 9216 + xcd * 1152)
    const char* ha;
    if (gs >= 0) {
#pragma unroll
      for (int i = 0; i < 2; ++i) { mg[i] = *(const u32x4*)(lds + L_W + wave * 2304 + i * 1024 + lane * 16); mh[i] = *(const u32*)(lds + L_W + wave * 2304 + 2048 + i * 128 + (lane >> 1) * 4); }
      lds_barrier();
#pragma unroll
      for (int j = 0; j < 4; ++j) dma(db + j * 8192, dv, lds_base + L_W + 73728 + (j * 8 + wave) * 1024);
      WAITV(6);
    } else if (oj >= 0) {
      BAR();
#pragma unroll
      for (int j = 0; j < NB; ++j) dma(db + j * 8 * 1152, dv ^ ((j & 1) * 64), lds_base + L_W + (wave * 8 + j) * 1024);
      asm volatile("s_waitcnt vmcnt(%0)" :: "n"(NB + 2) : "memory"); SCHED();
    } else {
      const int r_ = max(min(lane / 9, H1 - 1), 0), c_ = lane - (lane / 9) * 9;
      ha = HROW(r_ * 112 + idx * 8 + wave) + c_ * 128;
      if (lane >= H1 * 9) {
        const int k_ = min((idx * 8 + wave) * NS + min(lane - H1 * 9, NS - 1), 2 * (576 - NU) - 1), g0 = ((3 - (int)((uintptr_t)wu >> 7) - xcd * 9) & 7) + (k_ & 1) * 8;
        ha = (const char*)wu + (size_t)(k_ >> 1) * 9216 + (xcd * 9 + (g0 < 9 ? g0 : g0 - 8)) * 128;
      }
      asm volatile("" : "+v"(ha));
      WAITV(2);
    }
    u32x4 vf[8];
    asm volatile("s_nop 0 ; both branches above cover V up to the last two" : "+v"(vb[24]), "+v"(vb[25]), "+v"(vb[26]), "+v"(vb[27]), "+v"(vb[28]), "+v"(vb[29]));
#pragma unroll
    for (int t = 0; t < 8; ++t)
#pragma unroll
      for (int j = 0; j < 3; ++j) vf[t][j] = (t & 1) ? phi(vb[24 + 2 * j][t >> 1], vb[25 + 2 * j][t >> 1]) : plo(vb[24 + 2 * j][t >> 1], vb[25 + 2 * j][t >> 1]);
#pragma unroll
    for (int t = 0; t < 8; ++t) asm volatile("" : "+v"(vf[t][0]), "+v"(vf[t][1]), "+v"(vf[t][2]));
    if (gs >= 0) WAITV(4); else if (oj >= 0) WAITV(NB); else { WAITV(0); dma_keep64(ha, lds_base + L_B + 4096 + wave * 256); }
    asm volatile("s_nop 0 ; both branches above cover V" : "+v"(vb[30]), "+v"(vb[31]));
#pragma unroll
    for (int t = 0; t < 8; ++t) {
      vf[t][3] = (t & 1) ? phi(vb[30][t >> 1], vb[31][t >> 1]) : plo(vb[30][t >> 1], vb[31][t >> 1]);
      oa[t] = mfma(pf[3], vf[t], oa[t]);
    }
    STAMP(5); TSB(6);
    if (grp < 2) {
      *(f32x4*)(lds + mw_) = f32x4{oa[0][0], oa[1][0], oa[2][0], oa[3][0]};
      *(f32x4*)(lds + mw_ + 16) = f32x4{oa[4][0], oa[5][0], oa[6][0], oa[7][0]};
    }
    lds_barrier();
    if (wave == 0) {
      f32x4 m[8];
#pragma unroll
      for (int q = 0; q < 8; ++q) m[q] = *(const f32x4*)(lds + mr_ + q * 1024);
      SCHED();
      const f32x4 o = ((m[0] + m[1]) + (m[2] + m[3])) + ((m[4] + m[5]) + (m[6] + m[7]));
      st128(rs, oo, u32x4{sanf(o[0]), sanf(o[1]), sanf(o[2]), sanf(o[3])});
    }
    STAMP(6); TSB(7); SCHED();
  if (oj >= 0) {
    const u32 t6o_ = (u32)__builtin_amdgcn_s_memrealtime();
    int wa = L_W + (wave >> 2) * 32768 + (16 * (grp >> 1) + 8 * (n >> 3) + 4 * (grp & 1) + ((n >> 2) & 1)) * 128 + (((2 * (wave & 3) + ((lane >> 1) & 1)) ^ (4 * (n >> 3) + 2 * (grp & 1))) << 4) + (lane & 1) * 8;
    int iw = L_OI + wave * 1024 + (((lane >> 1) & 3) * 16 + ((4 * grp + 2 * (lane & 1) + (n >> 3)) ^ ((lane >> 1) & 1) ^ (lane & 4))) * 16;
    int ir = L_OI + (wave >> 2) * 4096 + (grp * 16 + (n ^ (grp & 1) ^ (4 * (grp >> 1)))) * 16, ix = (lane & 2) ? 0xeeee : 0x4444;
    int rw = L_OI + 8192 + (wave >> 2) * 2304 + grp * 576 + (wave & 3) * 64 + n * 4, rr = L_OI + 8192 + wave * 288 + ((n & 7) * 2 + (n >> 3)) * 16;
    int xo = W_XP + (wave * 9 + oj) * 2048 + (n >> 3) * 1024 + op * 128 + (n & 7) * 16;
    int ob = W_O + ((op * 2 + (wave >> 2)) * 8 + (lane >> 3)) * 2048 + (wave & 3) * 256 + (lane & 7) * 16;
    asm volatile("" : "+v"(rw), "+v"(rr), "+v"(xo), "+v"(ob), "+v"(iw), "+v"(ir), "+v"(wa), "+v"(ix));
    float xv[2], ga[2]; int xi, xq;
    if (wave == 7) {
      const int g = lane & 7;
      u32 mm[2];
#pragma unroll
      for (int i = 0; i < 2; ++i) {
        xv[i] = b2f(*(const u16*)(lds + L_B + 3200 + g * 16 + i * 8 + grp * 2));
        mm[i] = *(const u32*)(lds + L_B + 3072 + g * 16 + i * 8 + (grp >> 1) * 4);
      }
      _Pragma("nounroll") for (int sp = 0; sp < SPIN_CAP && !__all(mxu(mm[0], mm[1]) != 0xffffffffu); ++sp) {
        nap(4);
#pragma unroll
        for (int i = 0; i < 2; ++i) mm[i] = ld32(rs, W_M + op * 1152 + oj * 128 + g * 16 + i * 8 + (grp >> 1) * 4);
      }
      ga[0] = pick2(mm[0], grp); ga[1] = pick2(mm[1], grp);
      xi = W_XP + (op * 9 + oj) * 2048 + lane * 16; xq = W_X1 + ((lane >> 3) * 8 + op) * 1152 + oj * 128 + g * 16;
      asm volatile("" : "+v"(xv[0]), "+v"(xv[1]), "+v"(ga[0]), "+v"(ga[1]), "+v"(xi), "+v"(xq));
    }
    u32x4 a[4];
    UNTIL(t6o_ + OD);
#define OLD() do { _Pragma("unroll") for (int p = 0; p < 4; ++p) a[p] = ld128(rs, ob + (p >> 1) * 1024 + (p & 1) * 128); } while (0)
#define OOK() __all(mxu(mxu(mx4(a[0]), mx4(a[1])), mxu(mx4(a[2]), mx4(a[3]))) != 0xffffffffu)
    OLD();
    SCHED();
    WAITV(4); BAR();
    TSC(0);
    u32x8 bw[4];
    f32x4 acc = {0.f, 0.f, 0.f, 0.f};
#define WFRAG(s0_, s1_) do { _Pragma("unroll") for (int s = s0_; s < s1_; ++s) { \
      const u32x2 t0 = trd(lds, wa + s * 8192), t1 = trd(lds, (wa ^ 16) + 256 + s * 8192), t2 = trd(lds, wa + 4096 + s * 8192), t3 = trd(lds, (wa ^ 16) + 4352 + s * 8192); \
      bw[s] = u32x8{t0[0], t0[1], t1[0], t1[1], t2[0], t2[1], t3[0], t3[1]}; } } while (0)
    WFRAG(0, 4);
    asm volatile("" : "+v"(bw[0]), "+v"(bw[1]), "+v"(bw[2]), "+v"(bw[3]), "+v"(acc));
    asm volatile("" : "+v"(a[0]), "+v"(a[1]), "+v"(a[2]), "+v"(a[3]));
    if (!OOK()) {
      int sp = 0;
      _Pragma("nounroll") do { nap(2); OLD(); } while (!OOK() && ++sp < SPIN_CAP);
    }
    STAMP(7); TSC(1);
#define OS(p, j) (__uint_as_float(a[p][j]) + __uint_as_float(a[(p) + 2][j]))
    *(u32x4*)(lds + iw) = u32x4{cvt2(OS(0, 0), OS(0, 1)), cvt2(OS(0, 2), OS(0, 3)), cvt2(OS(1, 0), OS(1, 1)), cvt2(OS(1, 2), OS(1, 3))};
    lds_barrier();
    TSC(2);
    u32x4 of[4];
#pragma unroll
    for (int k = 0; k < 4; ++k) { of[k] = *(const u32x4*)(lds + ir + k * 1024); SCHED(); }
#pragma unroll
    for (int k = 0; k < 4; ++k) acc = smf(of[k], bw[k], acc, ix);
    const f32x2 e = smf_sum(acc);
    *(float*)(lds + rw) = e.x; *(float*)(lds + rw + 288) = e.y;
    lds_barrier();
    TSC(3);
    if (lane < 16) {
      const f32x4 r = *(const f32x4*)(lds + rr) + *(const f32x4*)(lds + rr + 2304);
      st128(rs, xo, u32x4{sanf(r[0]), sanf(r[1]), sanf(r[2]), sanf(r[3])});
    }
    TSC(4);
    if (wave == 7) {
      u32x4 xp[2];
      _Pragma("nounroll") for (int sp = 0; sp < SPIN_CAP; ++sp) {
        xp[0] = ld128(rs, xi); xp[1] = ld128(rs, xi + 1024);
        if (__all(mxu(mx4(xp[0]), mx4(xp[1])) != 0xffffffffu)) break;
        nap(1);
      }
      TSC(5);
      const u32x2 r0 = gather4(rbf(xv[0] + rbf(ga[0] * rbf(red8(asf4(xp[0])))))), r1 = gather4(rbf(xv[1] + rbf(ga[1] * rbf(red8(asf4(xp[1]))))));
      st128(rs, xq, u32x4{sanu(r0[0]), sanu(r0[1]), sanu(r1[0]), sanu(r1[1])});
      if (lane == 0) st128(rs, W_TS + (oj * 8 + op) * 128, u32x4{(u32)__builtin_amdgcn_s_memrealtime(), 0u, e0, e1});
      STAMPW(8); TSC(6);
    }
  }
  if (oj >= 0) STAMP(13);
  if (gs >= 0) {
    constexpr int L_H2 = L_B;
    constexpr int L_ACT = L_C + 4608;
    const int b = wave;
    u32x8 bw[9], wdf[5];
    f32x4 d[6];
    const u32 gv = (lane >> 3) * (FF * 2) + (((lane & 7) ^ ((wave & 1) * 4 + (lane >> 4))) << 4);
#define GDMA(j0, j1) do { _Pragma("unroll") for (int j = j0; j < j1; ++j) dma((const char*)(wg + gs * 64) + (size_t)(j * 64 + wave * 8) * (FF * 2), gv, lds_base + L_W + 73728 + (j * 8 + wave) * 1024); } while (0)
#define UDMA(j0, j1) do { _Pragma("unroll") for (int j = j0; j < j1; ++j) dma((const char*)(wu + gs * 64) + (size_t)(j * 64 + wave * 8) * (FF * 2), gv ^ 16u, lds_base + L_W + (j * 8 + wave) * 1024); } while (0)
    BAR(); SCHED();
    TSC(0);
    float sb[9], cb1[9];
    int ia[2], ra, wa, ix, yo[3], tw, tr_, xa, xb;
    u32x4 x8, fy; u32 x1;
    asm volatile("" : "=v"(fy));
    int fo = W_FY + min(lane, NPY / 2 - 1) * 16;
    {
#pragma unroll
      for (int j = 4; j < 9; ++j) dma(db + j * 8192, dv, lds_base + L_W + 73728 + (j * 8 + wave) * 1024);
      BAR();
      UDMA(0, UA);
    ia[0] = L_H2 + (lane >> 3) * 1024 + (((lane >> 1) & 3) * 16 + ((4 * (b >> 1) + 2 * (lane & 1) + (b & 1)) ^ ((lane >> 1) & 1) ^ (lane & 4))) * 16;
    ia[1] = L_H2 + 8192 + (grp * 16 + ((4 * (b >> 1) + 2 * (n >> 3) + (b & 1)) ^ (grp & 1) ^ (4 * (grp >> 1)))) * 16 + (lane & 7) * 2;
    ra = L_H2 + (grp * 16 + (n ^ (grp & 1) ^ (4 * (grp >> 1)))) * 16;
    wa = L_W + ((lane & 2) ? 0 : 73728) + (16 * (grp >> 1) + 8 * (n >> 3) + 4 * (grp & 1) + ((n >> 2) & 1)) * 128 + ((wave ^ (4 * (n >> 3) + 2 * (grp & 1)) ^ ((lane >> 1) & 1)) << 4) + (lane & 1) * 8;
    ix = (lane & 2) ? 0xeeee : 0x4444;
    tw = L_ACT + ((wave >> 1) * 16 + 4 * grp + (n & 2) + (n >> 3)) * 16 + ((n >> 2) & 1) * 8 + (wave & 1) * 4 + (n & 1) * 2; tr_ = L_ACT + lane * 16;
    yo[0] = W_Y + (((grp * 2) * 18 + wave * 2 + (n >> 3)) * NG + gs) * 128 + (n & 7) * 16;
    yo[1] = W_Y + ((grp * 2 * 18 + 16 + ((wave >> 1) & 1)) * NG + gs) * 128 + (wave & 1) * 64 + n * 4;
    yo[2] = yo[0] + 18 * NG * 128;
    xa = W_X1 + (xcd * 8 + b) * 1152 + lane * 16; xb = W_X1 + (xcd * 8 + b) * 1152 + 1024 + (lane >> 1) * 4;
    asm volatile("" : "+v"(ia[0]), "+v"(ia[1]), "+v"(ra), "+v"(wa), "+v"(ix), "+v"(tw), "+v"(tr_), "+v"(yo[0]), "+v"(yo[1]), "+v"(yo[2]), "+v"(xa), "+v"(xb), "+v"(fo));
#define MOK() __all(mxu(mxu(mx4(mg[0]), mx4(mg[1])), mxu(mh[0], mh[1])) != 0xffffffffu)
#define MLD(b_) do { _Pragma("unroll") for (int i = 0; i < 2; ++i) { mg[i] = ld128(rs, W_M + (8 + i * 8 + (b_)) * 1152 + lane * 16); mh[i] = ld32(rs, W_M + (8 + i * 8 + (b_)) * 1152 + 1024 + (lane >> 1) * 4); } } while (0)
#define MUNP() do { _Pragma("unroll") for (int i = 0; i < 4; ++i) { sb[2 * i] = lo16(mg[0][i]); sb[2 * i + 1] = hi16(mg[0][i]); \
                      cb1[2 * i] = rbf(1.f + lo16(mg[1][i])); cb1[2 * i + 1] = rbf(1.f + hi16(mg[1][i])); } \
                    sb[8] = pick2(mh[0], lane); cb1[8] = rbf(1.f + pick2(mh[1], lane)); \
                    _Pragma("unroll") for (int i = 0; i < 9; ++i) asm volatile("" : "+v"(sb[i]), "+v"(cb1[i])); } while (0)
      if (!MOK()) {
        int sp = 0;
        _Pragma("nounroll") do { nap(2); MLD(b); } while (!MOK() && ++sp < SPIN_CAP);
      }
      MUNP();
      WAITV(UA); BAR();
      {
        const int q = n >> 2, p = lane & 3, r = 16 * (grp >> 1) + 4 * (grp & 1) + q;
        const int a0 = L_W + 73728 + r * 1152 + (p & 1) * 8;
#pragma unroll
        for (int t = 0; t < 5; ++t) {
          const int a = t < 4 ? a0 + wave * 128 + (((t + (p >> 1) * 4) ^ (((r + wave) >> 1) & 3)) << 4) : a0 + 1024 + ((((wave & 3) * 2 + (p >> 1)) ^ ((r >> 1) & 3)) << 4);
          const u32x2 t0 = trd(lds, a), t1 = trd(lds, a + 9216), t2 = trd(lds, a + 36864), t3 = trd(lds, a + 36864 + 9216);
          wdf[t] = u32x8{t0[0], t0[1], t1[0], t1[1], t2[0], t2[1], t3[0], t3[1]};
        }
#pragma unroll
        for (int t = 0; t < 5; ++t) asm volatile("" : "+v"(wdf[t]));
      }
      TSC(1);
      UDMA(UA, 9);
      lds_barrier();
      GDMA(0, 9);
      WAITV(4); BAR();
      WFRAG(0, 5);
      asm volatile("" : "+v"(bw[0]), "+v"(bw[1]), "+v"(bw[2]), "+v"(bw[3]), "+v"(bw[4]));
#pragma unroll
      for (int t = 0; t < 6; ++t) { d[t] = f32x4{0.f, 0.f, 0.f, 0.f}; asm volatile("" : "+v"(d[t])); }
      TSC(2);
      WAITV(0);
      x8 = ld128(rs, xa); x1 = ld32(rs, xb);
      SCHED();
      BAR();
      WFRAG(5, 9);
      SCHED();
      STAMP(13);
    }
    {
      TSC(3);
#define XOK() __all(mxu(mx4(x8), x1) != 0xffffffffu)
      if (!XOK()) {
        int sp = 0;
        _Pragma("nounroll") do { nap(1); x8 = ld128(rs, xa); x1 = ld32(rs, xb); } while (!XOK() && ++sp < SPIN_CAP);
      }
      STAMP(9); TSC(4);
      if (wave == 0) fy = ld128(rs, fo);
      SCHED();
      u32x4 h8; u32 h1;
      ln9f(x8, pick2(x1, lane), cb1, sb, h8, h1);
      *(u32x4*)(lds + ia[0]) = h8;
      *(u16*)(lds + ia[1]) = (u16)h1;
      TSC(5);
      lds_barrier();
      u32x4 af[9];
#pragma unroll
      for (int s = 0; s < 9; ++s) { af[s] = *(const u32x4*)(lds + ra + s * 1024); SCHED(); }
      f32x4 acc = d[5];
#pragma unroll
      for (int s = 0; s < 9; ++s) acc = smf(af[s], bw[s], acc, ix);
      const f32x2 vv = smf_sum(acc);
      const int v0 = __float_as_int(vv.x), v1 = __float_as_int(vv.y);
      const float g = __int_as_float(__builtin_amdgcn_update_dpp(v0, v1, 0x128, 0xf, 0xc, false)), u = __int_as_float(__builtin_amdgcn_update_dpp(v1, v0, 0x128, 0xf, 0x3, false));
      const float gr = rbf(g);
      *(u16*)(lds + tw) = (u16)cvt2(rbf(gr * frcp(1.f + __expf(-gr))) * rbf(u), 0.f);
      SCHED();
      if (wave == 0 && !FOK(fy)) {
        int sp = 0;
        _Pragma("nounroll") do { nap(2); fy = ld128(rs, fo); } while (!FOK(fy) && ++sp < SPIN_CAP);
      }
    }
    lds_barrier();
    STAMP(10); TSC(6);
    {
      const u32x4 af = *(const u32x4*)(lds + tr_);
#pragma unroll
      for (int t = 0; t < 4; ++t) d[t] = smf(af, wdf[t], d[t], ix);
      SCHED();
      f32x2 e[5];
#pragma unroll
      for (int t = 0; t < 4; ++t) e[t] = smf_sum(d[t]);
#pragma unroll
      for (int j = 0; j < 2; ++j)
        st128(rs, yo[2 * j], u32x4{sanf(e[0][j]), sanf(e[1][j]), sanf(e[2][j]), sanf(e[3][j])});
      SCHED();
      if (wave < 4) {
        e[4] = smf_sum(smf(af, wdf[4], d[4], ix));
        __builtin_amdgcn_raw_buffer_store_b32(sanf(e[4][0]), rs, yo[1], 0, 16);
        __builtin_amdgcn_raw_buffer_store_b32(sanf(e[4][1]), rs, yo[1], 18 * NG * 128, 16);
      }
      TSC(7);
    }
  }
  {
  int t3_ = threadIdx.x; asm volatile("" : "+v"(t3_));
  const int tid = t3_, lane = tid & 63, n = lane & 15, grp = lane >> 4;
  const u32 t6_ = (u32)__builtin_amdgcn_s_memrealtime();
  if (idx >= 1 && idx < 14 && wave >= 2) {
    const int r_ = min(lane / 9, H2 - 1), c_ = lane - (lane / 9) * 9;
    ha = HROW(112 * H1 + ((wave - 2) * 13 + idx - 1) * H2 + r_) + c_ * 128;
    asm volatile("" : "+v"(ha));
    UNTIL(t6_ + HD2 + (wave - 2) * HS2);
    TSC(0);
    {
      const rsrc_t ry = __builtin_amdgcn_make_buffer_rsrc(ws + W_Y, 0, 144 * NG * 128, 0x00020000);
      const u32x4 ones = {0xffffffffu, 0xffffffffu, 0xffffffffu, 0xffffffffu};
      const int yp = (w - 8) * 13312 + (tid - 128) * 16;
      if (wave == 2) st128(ry, yp + 12288, ones);
      st128(ry, yp, ones); st128(ry, yp + 6144, ones);
      SCHED();
    }
    dma_keep64(ha, lds_base + L_B + 4096 + wave * 256);
    WAITV(1);
    if (lane == 0 && __hip_atomic_fetch_add((__attribute__((address_space(3))) u32*)((lds_t*)lds + L_B + 512), 1u, __ATOMIC_RELAXED, __HIP_MEMORY_SCOPE_WORKGROUP) == 5u)
      st64(rs, W_FY + (w - 8) * 8, u32x2{e0, e1});
    TSC(1);
    if (H3 > 0) {
      ha = HROW(112 * H1 + 78 * H2 + ((wave - 2) * 13 + idx - 1) * H3 + min(lane / 9, H3 - 1)) + c_ * 128;
      asm volatile("" : "+v"(ha));
      UNTIL(t6_ + HD3 + (wave - 2) * HS2);
      dma_keep64(ha, lds_base + L_B + 4096 + wave * 256);
    }
  }
  if (ml >= 0) {
    BAR();
    slab_dma(w_mod + ml * 64, NMOD * 2, lds_base + L_W, wave, lane);
    u32x4 wf[9], af[9];
    img_frags(lds, L_IMG, lane, af);
    asm volatile("s_waitcnt vmcnt(0)\n\ts_barrier" ::: "memory");
    slab_frags(lds, L_W, wave, lane, wf);
    const f32x4 s = half_sum(gemv9(wf, af));
    if (n < 8 && grp < 2) *(u32x2*)(lds + L_TILE + n * 128 + wave * 16 + grp * 8) = u32x2{cvt2(s[0], s[1]), cvt2(s[2], s[3])};
    lds_barrier();
    MPUB(ml);
  }

  if (idx >= 5 && idx < 23 && wave == 1) {
    const int b = xcd, jh = idx - 5, col = jh * 32 + (jh < 16 ? (lane & 7) + grp * 8 : (lane & 7) * 4 + grp);
    const rsrc_t rw_ = __builtin_amdgcn_make_buffer_rsrc((void*)w_r, 0, D * 2, 0x00020000);
    const u32x4 wr8 = __builtin_amdgcn_raw_buffer_load_b128(rw_, lane * 16, 0, 0);
    const float wr1 = b2f(__builtin_amdgcn_raw_buffer_load_b16(rw_, 1024 + lane * 2, 0, 0));
    u32x4 mg[2]; u32 mh[2];
    _Pragma("nounroll") for (int sp = 0; sp < SPIN_CAP; ++sp) { MLD(b); if (MOK()) break; nap(4); }
    float sb[9], cb1[9];
    MUNP();
    const int xa = W_X1 + (xcd * 8 + b) * 1152 + lane * 16, xb = W_X1 + (xcd * 8 + b) * 1152 + 1024 + (lane >> 1) * 4;
    u32x4 x8; u32 x1;
    _Pragma("nounroll") for (int sp = 0; sp < SPIN_CAP; ++sp) { x8 = ld128(rs, xa); x1 = ld32(rs, xb); if (XOK()) break; nap(1); }
    const u32 tx_ = (u32)__builtin_amdgcn_s_memrealtime();
    const int go = W_M + (24 + b) * 1152 + (col >> 1) * 4, fo = W_FY + min(lane, NPY / 2 - 1) * 16;
    u32 gq = ld32(rs, go); u32x4 fy = ld128(rs, fo);
    const float v8 = pick2(x1, lane);
    u32x4 h8; u32 h1;
    ln9(x8, v8, cb1, sb, h8, h1);
    unsigned char* row = lds + L_C;
    *(u32x4*)(row + lane * 16) = x8; *(u32x4*)(row + 1152 + lane * 16) = h8;
    *(u16*)(row + 1024 + lane * 2) = (u16)(__float_as_uint(v8) >> 16); *(u16*)(row + 2176 + lane * 2) = (u16)h1;
    float r = lo16(h1) * wr1;
#pragma unroll
    for (int i = 0; i < 4; ++i) r = fmaf(hi16(h8[i]), hi16(wr8[i]), fmaf(lo16(h8[i]), lo16(wr8[i]), r));
    r = rbf(wsum(r));
    float rsel = rbf(1.f / (1.f + __expf(-r)));
    float x1v = b2f(*(const u16*)(row + col * 2)), h2 = b2f(*(const u16*)(row + 1152 + col * 2));
    _Pragma("nounroll") for (int sp = 0; sp < SPIN_CAP && !(__all(gq != 0xffffffffu) && FOK(fy)); ++sp) { nap(2); gq = ld32(rs, go); fy = ld128(rs, fo); }
    float gbv = pick2(gq, col);
    u32 tgo;
    {
      const u32x4 tq = ld128(rs, W_TS + lane * 128);
      int d_ = tag_ok(tq, e0, e1) ? (int)(tq[0] - tx_) : 0;
      d_ = max(d_, (int)dppu<0xB1>(d_)); d_ = max(d_, (int)dppu<0x4E>(d_)); d_ = max(d_, (int)dppu<0x141>(d_)); d_ = max(d_, (int)dppu<0x140>(d_));
      d_ = max(max(__builtin_amdgcn_readlane(d_, 0), __builtin_amdgcn_readlane(d_, 16)), max(__builtin_amdgcn_readlane(d_, 32), __builtin_amdgcn_readlane(d_, 48)));
      tgo = tx_ + min(d_ + TGO, TGO - 15);
    }
    int yi = W_Y + (b * 18 + jh) * NG * 128 + lane * 16, oo = (b * D + col) * 2;
    asm volatile("" : "+v"(rsel), "+v"(x1v), "+v"(h2), "+v"(gbv), "+v"(yi), "+v"(oo));
    TSC(5);
    UNTIL(tgo);
    float z;
    _Pragma("nounroll") for (int sp = 0; sp < SPIN_CAP; ++sp) {
      u32x4 y[9];
#pragma unroll
      for (int i = 0; i < 9; ++i) y[i] = ld128(rs, yi + i * 1024);
      z = red8((((asf4(y[0]) + asf4(y[1])) + (asf4(y[2]) + asf4(y[3]))) + ((asf4(y[4]) + asf4(y[5])) + (asf4(y[6]) + asf4(y[7])))) + asf4(y[8]));
      if (__all(z == z)) break;
      u32 m = mx4(y[8]);
#pragma unroll
      for (int i = 0; i < 8; ++i) m = mxu(m, mx4(y[i]));
      if (__all(m != 0xffffffffu)) break;
      nap(1);
    }
    STAMPW(11); TSC(6);
    const float y2 = rbf(rbf(z) * rsel);
    const float res = x1v + rbf(gbv * rbf(h2 + y2));
    if (!(lane & 8)) __builtin_amdgcn_raw_buffer_store_b16((u16)(cvt2(res, 0.f) & 0xffffu), __builtin_amdgcn_make_buffer_rsrc(out, 0, 8 * D * 2, 0x00020000), oo, 0, 0);
    STAMPW(12); TSC(7);
  }
  TSOUT();
  }
  }
}

extern "C" void kernel_launch(void* const* d_in, const int* in_sizes, int n_in, void* d_out, int out_size, void* d_ws, size_t ws_size,
                              hipStream_t stream) {
  fused_kernel<<<NWG, NT, 0, stream>>>((const u16*)d_in[0], (const u16*)d_in[1], (const u16*)d_in[2], (const u16*)d_in[3], (const u16*)d_in[4],
                                       (const u16*)d_in[6], (const u16*)d_in[7], (const u16*)d_in[8], (const u16*)d_in[9], (const u16*)d_in[10],
                                       (const u16*)d_in[11], (unsigned char*)d_ws, (u16*)d_out);
}
```

```cpp
#include <hip/hip_runtime.h>
#include <hip/hip_bf16.h>
#include <cstdint>

typedef unsigned u32;
typedef unsigned short u16;
typedef u32 u32x4 __attribute__((ext_vector_type(4)));
typedef u32 u32x2 __attribute__((ext_vector_type(2)));
typedef float f32x4 __attribute__((ext_vector_type(4)));
typedef float f32x2 __attribute__((ext_vector_type(2)));
typedef short s16x4 __attribute__((ext_vector_type(4)));
typedef __bf16 bf16x8 __attribute__((ext_vector_type(8)));
typedef __bf16 bf16x16 __attribute__((ext_vector_type(16)));
typedef u32 u32x8 __attribute__((ext_vector_type(8)));
typedef __bf16 bf16x2 __attribute__((ext_vector_type(2)));
typedef __attribute__((address_space(3))) unsigned char lds_t;

constexpr int D = 576, SK = 8192, WIN = 2048, FF = 4608, NQ = 4096, NMOD = 6 * D;
constexpr int NWG = 256, NT = 512, NG = 72;
constexpr int SPIN_CAP = 1 << 15;

constexpr int W_O = 0;
constexpr int W_XP = W_O + 262144;
constexpr int W_X1 = W_XP + 147456;
constexpr int W_M = W_X1 + 73728;
constexpr int W_MS = W_M + 36864;
constexpr int W_Y = W_MS;
constexpr int W_FL = W_Y + 144 * NG * 128;
constexpr int W_FY = W_FL + 1024;
constexpr int W_H = W_FY + 1024;
constexpr int W_Q = W_H + 36864;
constexpr int W_ST = W_Q + 131072;
constexpr int W_TS = W_ST + 32768;
constexpr int W_END = W_TS + 72 * 128;
constexpr int PZ = (W_MS / 16 + NG - 1) / NG;
constexpr int NPY = 104;
static_assert(PZ <= NT && NPY * 13 * 1024 >= 144 * NG * 128, "poison split");

constexpr int L_W = 0;
constexpr int L_IMG = 73728;
constexpr int L_TILE = L_IMG + 9216;
constexpr int L_OI = 65536;
constexpr int L_B = 147456;
constexpr int L_C = 155648;
constexpr int LDS_BYTES = 163840;

#define STAMP(i) do {} while (0)
#define STAMPW(i) do {} while (0)

#define SCHED() __builtin_amdgcn_sched_barrier(0)
#define UNTIL(t) do { _Pragma("nounroll") while ((int)((u32)__builtin_amdgcn_s_memrealtime() - (t)) < 0) __builtin_amdgcn_s_sleep(1); asm volatile("" ::: "memory"); } while (0)
#define BAR() asm volatile("s_barrier" ::: "memory")
#define TS(i) do {} while (0)
#define TSOUT() do {} while (0)
#define TSA(i) do {} while (0)
#define TSB(i) do {} while (0)
#define TSC(i) do {} while (0)
#define WAITV_(n) do { asm volatile("s_waitcnt vmcnt(" #n ")" ::: "memory"); SCHED(); } while (0)
#define WAITV(n) WAITV_(n)
#define DK 5
#define DV DK
#define PRM(i, v) (v)
#define PR PRM(0, 111)
#define TX PRM(1, 190)
#define LF PRM(2, 55)
#define LO PRM(3, 20)
#define E60 PRM(9, 10)
#define E62 PRM(10, 20)
#define H1 PRM(11, 2)
#define H2 PRM(12, 5)
#define H3 PRM(13, 4)
#define HD2 PRM(14, 10)
#define HD3 PRM(15, 70)
#define HS2 PRM(16, 6)
#define OD PRM(17, 60)
#define TGO PRM(18, 190)
#define TSL PRM(19, 220)
#define NB 4
#define UA 5
#define PAT 13, 15, 11, 16, 9, 12, 14, 8, 10, 18, 17
constexpr int AB[11] = {PAT};

__device__ __forceinline__ float lo16(u32 u) { return __uint_as_float(u << 16); }
__device__ __forceinline__ float hi16(u32 u) { return __uint_as_float(u & 0xffff0000u); }
__device__ __forceinline__ float b2f(u16 u) { return __uint_as_float((u32)u << 16); }
__device__ __forceinline__ u32 cvt2(float a, float b) { return __builtin_bit_cast(u32, __builtin_convertvector(f32x2{a, b}, bf16x2)); }
__device__ __forceinline__ float rbf(float v) { return __uint_as_float(cvt2(0.f, v)); }
__device__ __forceinline__ u32 sanf(float f) { u32 v = __float_as_uint(f); return v < 0xfffffffeu ? v : 0xfffffffeu; }
__device__ __forceinline__ u32 sanu(u32 v) { return v < 0xfffffffeu ? v : 0xfffffffeu; }
__device__ __forceinline__ f32x4 asf4(u32x4 v) { return __builtin_bit_cast(f32x4, v); }
__device__ __forceinline__ f32x2 up2(u32 u) { return f32x2{lo16(u), hi16(u)}; }
__device__ __forceinline__ f32x2 rbf2(f32x2 v) { return f32x2{rbf(v.x), rbf(v.y)}; }
__device__ __forceinline__ void lds_barrier() { asm volatile("s_waitcnt lgkmcnt(0)\n\ts_barrier" ::: "memory"); }
__device__ __forceinline__ f32x4 mfma(u32x4 a, u32x4 b, f32x4 c) {
  return __builtin_amdgcn_mfma_f32_16x16x32_bf16(__builtin_bit_cast(bf16x8, a), __builtin_bit_cast(bf16x8, b), c, 0, 0, 0);
}
__device__ __forceinline__ f32x4 smf(u32x4 a, u32x8 b, f32x4 c, int ix) {
  return __builtin_amdgcn_smfmac_f32_16x16x64_bf16(__builtin_bit_cast(bf16x8, a), __builtin_bit_cast(bf16x16, b), c, ix, 0, 0);
}
__device__ __forceinline__ f32x2 smf_sum(f32x4 d) { return f32x2{d[0], d[1]} + f32x2{d[2], d[3]}; }
__device__ __forceinline__ u32 plo(u32 a, u32 b) { return __builtin_amdgcn_perm(b, a, 0x05040100u); }
__device__ __forceinline__ u32 phi(u32 a, u32 b) { return __builtin_amdgcn_perm(b, a, 0x07060302u); }

extern "C" __device__ __attribute__((const)) unsigned long long llvm_dispatch_id() __asm("llvm.amdgcn.dispatch.id");
typedef __amdgpu_buffer_rsrc_t rsrc_t;
__device__ __forceinline__ u32x4 ld128(rsrc_t rs, int off) { return __builtin_amdgcn_raw_buffer_load_b128(rs, off, 0, 16); }
__device__ __forceinline__ u32 ld32(rsrc_t rs, int off) { return __builtin_amdgcn_raw_buffer_load_b32(rs, off, 0, 16); }
__device__ __forceinline__ void st128(rsrc_t rs, int off, u32x4 v) { __builtin_amdgcn_raw_buffer_store_b128(v, rs, off, 0, 16); }
__device__ __forceinline__ void st64(rsrc_t rs, int off, u32x2 v) { __builtin_amdgcn_raw_buffer_store_b64(v, rs, off, 0, 16); }
__device__ __forceinline__ void dma(const void* gbase, u32 voff, unsigned lds_dst) {
  unsigned keep;
  asm volatile("s_mov_b32 %0, m0\n\ts_mov_b32 m0, %3\n\ts_nop 2\n\tglobal_load_lds_dwordx4 %1, %2 nt\n\ts_mov_b32 m0, %0"
               : "=&s"(keep) : "v"(voff), "s"(gbase), "s"(lds_dst) : "memory");
}
__device__ __forceinline__ void dma_keep(const void* gbase, u32 voff, unsigned lds_dst) {
  unsigned keep;
  asm volatile("s_mov_b32 %0, m0\n\ts_mov_b32 m0, %3\n\ts_nop 2\n\tglobal_load_lds_dword %1, %2\n\ts_mov_b32 m0, %0"
               : "=&s"(keep) : "v"(voff), "s"(gbase), "s"(lds_dst) : "memory");
}
__device__ __forceinline__ void dma_keep64(const void* addr, unsigned lds_dst) {
  unsigned keep;
  asm volatile("s_mov_b32 %0, m0\n\ts_mov_b32 m0, %2\n\ts_nop 2\n\tglobal_load_lds_dword %1, off\n\ts_mov_b32 m0, %0"
               : "=&s"(keep) : "v"(addr), "s"(lds_dst) : "memory");
}
__device__ __forceinline__ void dma_sc(const void* gbase, u32 voff, unsigned lds_dst) {
  unsigned keep;
  asm volatile("s_mov_b32 %0, m0\n\ts_mov_b32 m0, %3\n\ts_nop 2\n\tglobal_load_lds_dwordx4 %1, %2 sc1\n\ts_mov_b32 m0, %0"
               : "=&s"(keep) : "v"(voff), "s"(gbase), "s"(lds_dst) : "memory");
}
__device__ __forceinline__ void ald128sc(u32x4& d, const void* gbase, u32 voff) { unsigned long long t; asm volatile("s_mov_b64 %1, %3\n\tglobal_load_dwordx4 %0, %2, %1 sc1" : "=v"(d), "=&s"(t) : "v"(voff), "s"(gbase) : "memory"); }
__device__ __forceinline__ u32x2 trd(const unsigned char* lds, int off) {
  return __builtin_bit_cast(u32x2, __builtin_amdgcn_ds_read_tr16_b64_v4i16((__attribute__((address_space(3))) s16x4*)((lds_t*)lds + off)));
}
#define nap(n) do { __builtin_amdgcn_s_sleep(n); asm volatile("" ::: "memory"); } while (0)
__device__ __forceinline__ float frcp(float v) { return __builtin_amdgcn_rcpf(v); }
__device__ __forceinline__ float fdiv(float a, float d, float r) { const float q = a * r; return fmaf(fmaf(-q, d, a), r, q); }
__device__ __forceinline__ float silu(float v) { const float d = 1.f + __expf(-v); const float r = frcp(d); return d < 3.0e38f ? fdiv(v, d, r) : v * r; }
template <int CTRL> __device__ __forceinline__ float dppf(float x) {
  return __builtin_bit_cast(float, __builtin_amdgcn_mov_dpp(__builtin_bit_cast(int, x), CTRL, 0xf, 0xf, true));
}
template <int CTRL> __device__ __forceinline__ u32 dppu(u32 x) { return (u32)__builtin_amdgcn_mov_dpp((int)x, CTRL, 0xf, 0xf, true); }
__device__ __forceinline__ u32 mxu(u32 a, u32 b) { return a > b ? a : b; }
__device__ __forceinline__ u32 mx4(u32x4 v) { return mxu(mxu(v[0], v[1]), mxu(v[2], v[3])); }
__device__ __forceinline__ float rl(float v, int l) { return __builtin_bit_cast(float, __builtin_amdgcn_readlane(__builtin_bit_cast(int, v), l)); }
__device__ __forceinline__ float wsum(float v) {
  v += dppf<0xB1>(v); v += dppf<0x4E>(v); v += dppf<0x141>(v); v += dppf<0x140>(v);
  return (rl(v, 0) + rl(v, 16)) + (rl(v, 32) + rl(v, 48));
}
__device__ __forceinline__ float up32(float v) { const u32 u = __float_as_uint(v); return __uint_as_float(__builtin_amdgcn_permlane32_swap(u, u, false, false)[1]); }
__device__ __forceinline__ float xsum32(float v) { const u32 u = __float_as_uint(v); const auto r = __builtin_amdgcn_permlane32_swap(u, u, false, false); return __uint_as_float(r[0]) + __uint_as_float(r[1]); }
__device__ __forceinline__ float xsum16(float v) { const u32 u = __float_as_uint(v); const auto r = __builtin_amdgcn_permlane16_swap(u, u, false, false); return __uint_as_float(r[0]) + __uint_as_float(r[1]); }
__device__ __forceinline__ float xmax32(float v) { const u32 u = __float_as_uint(v); const auto r = __builtin_amdgcn_permlane32_swap(u, u, false, false); return fmaxf(__uint_as_float(r[0]), __uint_as_float(r[1])); }
__device__ __forceinline__ float xmax16(float v) { const u32 u = __float_as_uint(v); const auto r = __builtin_amdgcn_permlane16_swap(u, u, false, false); return fmaxf(__uint_as_float(r[0]), __uint_as_float(r[1])); }
__device__ __forceinline__ float red8(f32x4 v) {
  const auto a = __builtin_amdgcn_permlane32_swap(__float_as_uint(v[0]), __float_as_uint(v[2]), false, false);
  const auto b = __builtin_amdgcn_permlane32_swap(__float_as_uint(v[1]), __float_as_uint(v[3]), false, false);
  const float w0 = __uint_as_float(a[0]) + __uint_as_float(a[1]), w1 = __uint_as_float(b[0]) + __uint_as_float(b[1]);
  const auto c = __builtin_amdgcn_permlane16_swap(__float_as_uint(w0), __float_as_uint(w1), false, false);
  const float z = __uint_as_float(c[0]) + __uint_as_float(c[1]);
  return z + dppf<0x128>(z);
}
__device__ __forceinline__ u32x2 gather4(float z) {
  const u32 u = __float_as_uint(z);
  const auto a = __builtin_amdgcn_permlane16_swap(u, u, false, false);
  const u32 pk = phi(a[0], a[1]);
  const auto b = __builtin_amdgcn_permlane32_swap(pk, pk, false, false);
  return u32x2{b[0], b[1]};
}
__device__ __forceinline__ f32x4 half_sum(f32x4 d) {
  f32x4 r;
#pragma unroll
  for (int j = 0; j < 4; ++j) r[j] = d[j] + dppf<0x108>(up32(d[j]));
  return r;
}
__device__ __forceinline__ float half_sum1(f32x4 d, bool lo) {
  const auto a = __builtin_amdgcn_permlane32_swap(__float_as_uint(d[0]), __float_as_uint(d[2]), false, false);
  const auto b = __builtin_amdgcn_permlane32_swap(__float_as_uint(d[1]), __float_as_uint(d[3]), false, false);
  return __uint_as_float(lo ? a[0] : b[1]) + dppf<0x128>(__uint_as_float(lo ? b[0] : a[1]));
}
__device__ __forceinline__ void unpack8(u32x4 u, float* f) {
#pragma unroll
  for (int j = 0; j < 4; ++j) { f[2 * j] = lo16(u[j]); f[2 * j + 1] = hi16(u[j]); }
}
__device__ __forceinline__ void ln_row(const float (&v)[16], bool has2, float& mean, float& den) {
  float s = 0.f;
#pragma unroll
  for (int i = 0; i < 8; ++i) s += v[i];
  if (has2) {
#pragma unroll
    for (int i = 8; i < 16; ++i) s += v[i];
  }
  s = wsum(s);
  const float mu = s * (1.f / D);
  mean = rbf(mu);
  float q = 0.f;
#pragma unroll
  for (int i = 0; i < 8; ++i) { const float t = v[i] - mu; q = fmaf(t, t, q); }
  if (has2) {
#pragma unroll
    for (int i = 8; i < 16; ++i) { const float t = v[i] - mu; q = fmaf(t, t, q); }
  }
  q = wsum(q);
  const float var = rbf(q * (1.f / D));
  den = rbf(sqrtf(rbf(var + 1e-5f)));
}
__device__ __forceinline__ float pick2(u32 t, int e) { return (e & 1) ? hi16(t) : lo16(t); }
__device__ __forceinline__ void ln9(u32x4 x8, float v8, const float (&cb1)[9], const float (&sb)[9], u32x4& h8, u32& h1) {
  f32x2 v[4];
#pragma unroll
  for (int i = 0; i < 4; ++i) v[i] = up2(x8[i]);
  const f32x2 s2 = (v[0] + v[1]) + (v[2] + v[3]);
  const float mu = wsum((s2.x + s2.y) + v8) * (1.f / D);
  const f32x2 mu2 = {mu, mu};
  const float t8 = v8 - mu;
  f32x2 q = {t8 * t8, 0.f};
#pragma unroll
  for (int i = 0; i < 4; ++i) { const f32x2 t = v[i] - mu2; q = __builtin_elementwise_fma(t, t, q); }
  const float mean = rbf(mu);
  f32x2 d[4];
#pragma unroll
  for (int i = 0; i < 4; ++i) d[i] = rbf2(v[i] - f32x2{mean, mean});
  const float d8 = rbf(v8 - mean);
  const float var = rbf(wsum(q.x + q.y) * (1.f / D));
  const float rden = frcp(rbf(__builtin_amdgcn_sqrtf(rbf(var + 1e-5f))));
#pragma unroll
  for (int i = 0; i < 4; ++i) {
    const f32x2 t = rbf2(rbf2(d[i] * f32x2{rden, rden}) * f32x2{cb1[2 * i], cb1[2 * i + 1]}) + f32x2{sb[2 * i], sb[2 * i + 1]};
    h8[i] = cvt2(t.x, t.y);
  }
  h1 = cvt2(rbf(rbf(d8 * rden) * cb1[8]) + sb[8], 0.f);
}
__device__ __forceinline__ void ln9f(u32x4 x8, float v8, const float (&cb1)[9], const float (&sb)[9], u32x4& h8, u32& h1) {
  const float s0 = lo16(__builtin_amdgcn_readfirstlane(x8[0]));
  f32x2 d[4];
#pragma unroll
  for (int i = 0; i < 4; ++i) d[i] = up2(x8[i]) - f32x2{s0, s0};
  const float d8 = v8 - s0;
  const f32x2 s2 = (d[0] + d[1]) + (d[2] + d[3]);
  const f32x2 q2 = __builtin_elementwise_fma(d[0], d[0], d[1] * d[1]) + __builtin_elementwise_fma(d[2], d[2], d[3] * d[3]);
  const auto r = __builtin_amdgcn_permlane32_swap(__float_as_uint((s2.x + d8) + s2.y), __float_as_uint(fmaf(d8, d8, q2.x) + q2.y), false, false);
  float z = __uint_as_float(r[0]) + __uint_as_float(r[1]);
  z += dppf<0xB1>(z); z += dppf<0x4E>(z); z += dppf<0x141>(z); z += dppf<0x140>(z);
  f32x2 t[4];
#pragma unroll
  for (int i = 0; i < 4; ++i) t[i] = d[i] * f32x2{cb1[2 * i], cb1[2 * i + 1]};
  const float md = (rl(z, 0) + rl(z, 16)) * (1.f / D);
  const float var = rbf(fmaf(-md, md, (rl(z, 32) + rl(z, 48)) * (1.f / D)));
  const float mr = s0 - rbf(s0 + md);
  const float rden = frcp(rbf(__builtin_amdgcn_sqrtf(rbf(var + 1e-5f))));
#pragma unroll
  for (int i = 0; i < 4; ++i) {
    const f32x2 u = __builtin_elementwise_fma(__builtin_elementwise_fma(f32x2{mr, mr}, f32x2{cb1[2 * i], cb1[2 * i + 1]}, t[i]), f32x2{rden, rden}, f32x2{sb[2 * i], sb[2 * i + 1]});
    h8[i] = cvt2(u.x, u.y);
  }
  h1 = cvt2(fmaf((d8 + mr) * cb1[8], rden, sb[8]), 0.f);
}
template <int J0 = 0, int J1 = 9>
__device__ __forceinline__ void slab_dma(const u16* base, int stride_b, unsigned lds_dst, int wave, int lane) {
  const u32 v0 = (lane >> 3) * stride_b + (((lane & 7) ^ ((wave & 1) * 4 + (lane >> 4))) << 4), v1 = v0 ^ 32u;
  const u32 v4 = wave >= 4 ? v1 : v0;
  const char* gb = (const char*)base + (size_t)wave * 8 * stride_b;
#pragma unroll
  for (int j = J0; j < J1; ++j) dma(gb + (size_t)j * 64 * stride_b, j < 4 ? v0 : j == 4 ? v4 : v1, lds_dst + (j * 8 + wave) * 1024);
}
__device__ __forceinline__ void slab_frags(const unsigned char* lds, int base, int wave, int lane, u32x4 (&wf)[9]) {
  const int g = lane >> 4, q = (lane & 15) >> 2, p = lane & 3, hf = p >> 1;
  const int a0 = base + (hf * 288 + 8 * g + q) * 128 + ((wave ^ ((g & 1) * 4 + (q >> 1)) ^ (hf * 2)) << 4) + (p & 1) * 8, a1 = (a0 ^ 32) + 512;
#pragma unroll
  for (int s = 0; s < 9; ++s) {
    const u32x2 lo = trd(lds, a0 + s * 4096), hi = trd(lds, a1 + s * 4096);
    wf[s] = u32x4{lo[0], lo[1], hi[0], hi[1]};
  }
}
__device__ __forceinline__ void img_frags(const unsigned char* lds, int img, int lane, u32x4 (&af)[9]) {
#pragma unroll
  for (int s = 0; s < 9; ++s) af[s] = *(const u32x4*)(lds + img + (s * 64 + lane) * 16);
}
__device__ __forceinline__ f32x4 gemv9(const u32x4 (&wf)[9], const u32x4 (&af)[9]) {
  f32x4 acc[3];
  SCHED();
#pragma unroll
  for (int s = 0; s < 9; ++s) acc[s % 3] = mfma(wf[s], af[s], s < 3 ? f32x4{0.f, 0.f, 0.f, 0.f} : acc[s % 3]);
  return (acc[0] + acc[1]) + acc[2];
}
__device__ __forceinline__ int img_off(int b, int k8) {
  const int hf = k8 >= 36 ? 1 : 0, kk = k8 - hf * 36;
  return ((kk >> 2) * 64 + (kk & 3) * 16 + hf * 8 + b) * 16;
}
__device__ __forceinline__ u32 tag_x(u32x4 v, u32 e0, u32 e1) { return (v[2] ^ e0) | (v[3] ^ e1); }
__device__ __forceinline__ bool tag_ok(u32x4 v, u32 e0, u32 e1) { return tag_x(v, e0, e1) == 0u; }

constexpr int CODE_BYTES = 27648;

__global__ void __launch_bounds__(NT, 2) fused_kernel(const u16* __restrict__ x, const u16* __restrict__ kc, const u16* __restrict__ vc,
                                                      const u16* __restrict__ wq, const u16* __restrict__ wo, const u16* __restrict__ w_r,
                                                      const u16* __restrict__ wg, const u16* __restrict__ wu, const u16* __restrict__ wd,
                                                      const u16* __restrict__ c, const u16* __restrict__ w_mod, unsigned char* ws, u16* out) {
  __shared__ __attribute__((aligned(16))) unsigned char lds[LDS_BYTES];
  int w = blockIdx.x;
  const int tid = threadIdx.x, lane = tid & 63, wave = __builtin_amdgcn_readfirstlane(tid >> 6);
  const int n = lane & 15, grp = lane >> 4;
  const size_t pairbase = ((size_t)(w >> 1) * SK + (SK - WIN)) * 256 + ((w & 1) << AB[8]);
  const int wpart = ((wave & 1) << AB[5]) + (((wave >> 1) & 1) << AB[6]) + ((wave >> 2) << AB[7]);
  int ko[4];
  ko[0] = wpart + (((n >> 2) & 1) << AB[0]) + ((n >> 3) << AB[1]) + grp * 64 + (n & 3) * 16;
  ko[1] = ko[0] + (1 << AB[2]); ko[2] = ko[0] + (1 << AB[3]); ko[3] = ko[0] + (1 << AB[2]) + (1 << AB[3]);
  const char* kp = (const char*)kc + pairbase;
  u32x4 kb[32], vb[32];
#define KLD(u) do { unsigned long long t_; asm volatile("s_mov_b64 %1, %3\n\tglobal_load_dwordx4 %0, %2, %1 nt" : "=v"(kb[u]), "=&s"(t_) : "v"(ko[(u) & 3]), "s"(kp + ((((u) >> 2) & 1) << AB[4]) + ((((u) >> 3) & 1) << AB[9]) + (((u) >> 4) << AB[10])) : "memory"); } while (0)
  unsigned long long tst;
  asm volatile("s_memrealtime %0\n\tglobal_load_dwordx4 %1, %5, %9 nt\n\tglobal_load_dwordx4 %2, %6, %9 nt\n\t"
               "global_load_dwordx4 %3, %7, %9 nt\n\tglobal_load_dwordx4 %4, %8, %9 nt"
               : "=&s"(tst), "=&v"(kb[0]), "=&v"(kb[1]), "=&v"(kb[2]), "=&v"(kb[3]) : "v"(ko[0]), "v"(ko[1]), "v"(ko[2]), "v"(ko[3]), "s"(kp) : "memory");
  asm volatile("s_waitcnt lgkmcnt(0)" : "+s"(w), "+s"(ws), "+s"(tst));
  const u32 tstart = (u32)tst;
  const int xcd = w & 7, idx = w >> 3;
  const rsrc_t rs = __builtin_amdgcn_make_buffer_rsrc(ws, 0, W_END, 0x00020000);
  const unsigned lds_base = (unsigned)(uintptr_t)(lds_t*)lds;
  const unsigned long long epoch = (llvm_dispatch_id() + 1ull) * 0x9E3779B97F4A7C15ull;
  const u32 e0 = (u32)epoch, e1 = (u32)(epoch >> 32);
  unsigned long long pc0; asm volatile("s_getpc_b64 %0" : "=s"(pc0));
  asm volatile("" : "+s"(vc), "+s"(w_r), "+s"(wu), "+s"(wd), "+s"(out), "+s"(x), "+s"(w_mod), "+s"(wg), "+s"(wq), "+s"(wo));

  const int me = idx < 6 ? xcd * 6 + idx : -1;
  const int ml = idx == 0 && xcd < 6 ? 48 + xcd : -1;
  const int qs = idx >= 6 && idx < 14 ? xcd * 8 + idx - 6 : -1;
  const int oj = idx >= 14 && idx < 23 ? idx - 14 : -1, op = xcd;
  const int gs = idx >= 23 ? xcd * 9 + idx - 23 : -1;

  const int s_att = w & 1, kvh = (w >> 1) & 15, b_att = w >> 5;
  {
    const char* vp = (const char*)vc + pairbase;
    int vo[4];
#define VLD(u) do { unsigned long long t_; asm volatile("s_mov_b64 %1, %3\n\tglobal_load_dwordx4 %0, %2, %1 nt" : "=v"(vb[u]), "=&s"(t_) : "v"(vo[(u) & 3]), "s"(vp + ((((u) >> 2) & 1) << AB[4]) + ((((u) >> 3) & 1) << AB[9]) + (((u) >> 4) << AB[10])) : "memory"); } while (0)
#define TIE8(a, o) asm volatile("" : "+v"(a[o]), "+v"(a[(o) + 1]), "+v"(a[(o) + 2]), "+v"(a[(o) + 3]), "+v"(a[(o) + 4]), "+v"(a[(o) + 5]), "+v"(a[(o) + 6]), "+v"(a[(o) + 7]))
    const int pr_ = PR;
    const u32 tp0 = tstart + TX - (gs >= 0 ? LF : oj >= 0 ? LO : 0);
#define PACE(i) do { _Pragma("nounroll") while ((int)((u32)__builtin_amdgcn_s_memrealtime() - tp0) < (i) * pr_ / 4) __builtin_amdgcn_s_sleep(2); SCHED(); } while (0)
    static_assert(DK <= 7 && DV <= 7, "the hand-placed waits assume it");
#define POISON() do { if (gs >= 0) { const int g_ = gs * PZ + tid; \
      if (tid < PZ && g_ < W_MS / 16) st128(rs, g_ * 16, u32x4{0xffffffffu, 0xffffffffu, 0xffffffffu, 0xffffffffu}); } SCHED(); } while (0)
#define PFLAG() do { if (gs >= 0 && tid == 448) st64(rs, W_FL + gs * 8, u32x2{e0, e1}); SCHED(); } while (0)
#define FOK(a) __all(((a[0] ^ e0) | (a[1] ^ e1) | tag_x(a, e0, e1)) == 0u)
#define FREQ() do { if (wave == 1) dma_sc(ws + W_FL, min(lane, NG / 2 - 1) * 16, lds_base + L_B + 2048); } while (0)
#define FCHK() do { if (wave == 1 && !FOK((*(const u32x4*)(lds + L_B + 2048 + lane * 16)))) { \
      _Pragma("nounroll") for (int sp = 0; sp < SPIN_CAP; ++sp) { nap(4); const u32x4 a_ = ld128(rs, W_FL + min(lane, NG / 2 - 1) * 16); if (FOK(a_)) break; } } SCHED(); } while (0)
#define SILU_IMAGE(c0, c1) do { float f[8]; unpack8(c0, f); u32x4 o; \
        _Pragma("unroll") for (int j = 0; j < 4; ++j) o[j] = cvt2(silu(f[2 * j]), silu(f[2 * j + 1])); \
        const int b_ = tid / 72, ch_ = tid - b_ * 72; \
        *(u32x4*)(lds + L_IMG + img_off(b_, ch_)) = o; \
        if (wave == 0) { unpack8(c1, f); \
          _Pragma("unroll") for (int j = 0; j < 4; ++j) o[j] = cvt2(silu(f[2 * j]), silu(f[2 * j + 1])); \
          *(u32x4*)(lds + L_IMG + img_off(7, 8 + lane)) = o; } } while (0)
    {
      constexpr int L_LAND = 98304;
      const unsigned land = lds_base + L_LAND + wave * 6144;
      const unsigned char* lp = lds + L_LAND + wave * 6144 + lane * 16;
      if (me >= 0) {
        dma_sc(c, tid * 16, land);
        if (wave == 0) dma_sc(c, (512 + lane) * 16, land + 1024);
        if (me >= 9 && me < 18) { dma(x, wave * 1152 + lane * 16, land + 2048); dma(x, wave * 1152 + 1024 + (lane & 7) * 16, land + 3072); }
      }
      {
        const int cnt = idx < 6 ? 2 : oj >= 0 ? 1 : 3, r0 = (idx < 6 ? idx * 16 : idx < 14 ? idx * 24 - 48 : idx < 23 ? idx * 8 + 176 : idx * 24 - 192) + wave * cnt;
        const int r_ = (lane * 37) >> 9, c_ = lane - r_ * 14;
        const char* ha = c_ < 8 ? (const char*)wq + (size_t)(r0 + r_) * (NQ * 2) + xcd * 1024 + c_ * 128 : (const char*)w_mod + (size_t)(r0 + r_) * (NMOD * 2) + (xcd * 6 + c_ - 8) * 128;
        if (r_ < cnt) dma_keep64(ha, land + 4096);
      }
      POISON();
      if (oj >= 0 && oj < 8 && wave == 4)
        dma_keep((const void*)pc0, min((oj & 3) * 8192 + lane * 128, CODE_BYTES - 128), land);
      const u32 v0 = (lane >> 3) * 1152 + (((lane & 7) ^ (lane >> 4)) << 4);
      const char* gb = (const char*)(wo + (size_t)(op * 512 + wave * 64) * D + oj * 64);
      const bool fs = qs >= 0 || me >= 0;
      const u16* sbase = qs >= 0 ? wq + qs * 64 : w_mod + me * 64;
      const int sstr = qs >= 0 ? NQ * 2 : NMOD * 2;
      const unsigned sdst = lds_base + L_W;
      if (oj >= 0) {
#pragma unroll
        for (int j = NB; j < 8; ++j) dma(gb + j * 8 * 1152, v0 ^ ((j & 1) * 64), lds_base + L_W + (wave * 8 + j) * 1024);
      }
#define WK(j) do { if (!((j) & 1)) PACE((j) + 4); KLD((j) + 4); WAITV(DK); } while (0)
      static_assert(DK <= 5, "six younger requests cover the poison");
      TSA(0);
      float* st = (float*)(lds + L_TILE + 1024);
      if (me >= 0) {
        WAITV(1);
        const u32x4 c0 = *(const u32x4*)lp, c1 = *(const u32x4*)(lp + 1024); SILU_IMAGE(c0, c1);
        if (me >= 9 && me < 18) {
          float v[16]; unpack8(*(const u32x4*)(lp + 2048), v); unpack8(*(const u32x4*)(lp + 3072), v + 8);
          float mean, den; ln_row(v, lane < 8, mean, den);
          if (lane == 0) { st[wave] = mean; st[8 + wave] = den; }
        }
      }
      TSA(1);
      if (fs) { lds_barrier(); UNTIL(tstart + TSL); slab_dma(sbase, sstr, sdst, wave, lane); BAR(); }
      WK(0);
      KLD(5);
      if (me >= 0) {
        u32x4 wf[9], af[9];
        img_frags(lds, L_IMG, lane, af);
        WAITV(2); BAR();
        TSA(2);
        slab_frags(lds, L_W, wave, lane, wf);
        f32x4 r = half_sum(gemv9(wf, af));
        if (me >= 18) { if (n < 8 && grp < 2) *(u32x2*)(lds + L_TILE + n * 128 + wave * 16 + grp * 8) = u32x2{cvt2(r[0], r[1]), cvt2(r[2], r[3])}; }
        else {
          const int k8 = (me >= 9 ? me - 9 : me) * 8 + wave;
          if (me >= 9) {
            const u32x2 xc = *(const u32x2*)(lds + L_LAND + (n & 7) * 6144 + 2048 + k8 * 16 + (grp & 1) * 8);
            const float mean = st[n & 7], rd = frcp(st[8 + (n & 7)]);
            const float xv[4] = {lo16(xc[0]), hi16(xc[0]), lo16(xc[1]), hi16(xc[1])};
#pragma unroll
            for (int j = 0; j < 4; ++j) r[j] = rbf(rbf(xv[j] - mean) * rd) * rbf(1.f + rbf(r[j]));
          }
          if (n < 8 && grp < 2) st128(rs, W_H + (me >= 9 ? 18432 : 0) + img_off(n, k8) * 2 + grp * 16, u32x4{cvt2(r[0], r[1]), cvt2(r[2], r[3]), e0, e1});
          STAMP(14);
        }
        TSA(3);
      } else WAITV(DK);
      WK(2); WK(3); WK(4); WK(5);
      if (gs >= 0) BAR();
      STAMP(2);
      PFLAG();
      WK(6); WK(7);
      PACE(12); KLD(12); WAITV(DK); KLD(13); WAITV(DK);
      PACE(14); KLD(14); WAITV(DK); KLD(15); WAITV(DK);
      const int hg0 = (wave * 144 + lane) * 16, hg2 = (wave * 144 + 128 + n) * 16;
#define HREQ() do { dma_sc(ws + W_H, hg0, land); dma_sc(ws + W_H + 18432, hg0, land + 1024); dma_sc(ws + W_H + 1024, hg0, land + 2048); \
                    dma_sc(ws + W_H + 18432 + 1024, hg0, land + 3072); dma_sc(ws + W_H, hg2, land + 4096); dma_sc(ws + W_H + 18432, hg2, land + 5120); } while (0)
#define HGET() do { _Pragma("unroll") for (int i = 0; i < 6; ++i) hq[i] = *(const u32x4*)(lp + i * 1024); } while (0)
#define HQOK() __all((tag_x(hq[0], e0, e1) | tag_x(hq[1], e0, e1) | tag_x(hq[2], e0, e1) | tag_x(hq[3], e0, e1) | tag_x(hq[4], e0, e1) | tag_x(hq[5], e0, e1)) == 0u)
      PACE(16); KLD(16); WAITV(DK); KLD(17); WAITV(DK); PACE(18); KLD(18); WAITV(DK); KLD(19); WAITV(DK);
      PACE(20); KLD(20); WAITV(DK); KLD(21); WAITV(DK);
      TSA(4);
      if (qs >= 0) HREQ();
      PACE(22); FREQ(); KLD(22); WAITV(DK); KLD(23); WAITV(DK); PACE(24); KLD(24); WAITV(DK); KLD(25); WAITV(DK);
      if (qs >= 0) WAITV(4);
      TSA(5);
      PACE(26); KLD(26); KLD(27);
      if (qs >= 0) {
        u32x4 hq[6]; HGET();
        _Pragma("nounroll") for (int sp = 0; sp < SPIN_CAP && !HQOK(); ++sp) { nap(16); HREQ(); WAITV(0); HGET(); }
        TSA(6);
#define HSUM(a, b) cvt2(lo16(a) + lo16(b), hi16(a) + hi16(b))
        *(u32x2*)(lds + L_IMG + hg0 / 2) = u32x2{HSUM(hq[0][0], hq[1][0]), HSUM(hq[0][1], hq[1][1])};
        *(u32x2*)(lds + L_IMG + 512 + hg0 / 2) = u32x2{HSUM(hq[2][0], hq[3][0]), HSUM(hq[2][1], hq[3][1])};
        if (lane < 16) *(u32x2*)(lds + L_IMG + hg2 / 2) = u32x2{HSUM(hq[4][0], hq[5][0]), HSUM(hq[4][1], hq[5][1])};
      } else { WAITV(DK); PACE(28); }
      KLD(28); KLD(29);
      if (qs >= 0) {
        u32x4 wf[9], af[9];
        lds_barrier();
        slab_frags(lds, L_W, wave, lane, wf);
        img_frags(lds, L_IMG, lane, af);
        const f32x4 s = half_sum(gemv9(wf, af));
        if (n < 8 && grp < 2) *(u32x2*)(lds + L_TILE + n * 128 + wave * 16 + grp * 8) = u32x2{cvt2(s[0], s[1]), cvt2(s[2], s[3])};
      } else WAITV(DK);
      PACE(30); KLD(30); KLD(31);
      WAITV(9); FCHK();
#define MPUB(m_) do { if (wave == 0) { const u32x4 t_ = *(const u32x4*)(lds + L_TILE + lane * 16); const int ch_ = ((m_) - 18) / 9, sl_2 = (m_) - 18 - ch_ * 9; \
          st128(rs, W_M + (ch_ * 8 + (lane >> 3)) * 1152 + sl_2 * 128 + (lane & 7) * 16, u32x4{sanu(t_[0]), sanu(t_[1]), sanu(t_[2]), sanu(t_[3])}); } } while (0)
      if (qs >= 0 || me >= 18) {
        lds_barrier();
        if (qs < 0) MPUB(me);
        else if (wave < 2) {
          const u32x2 t2 = *(const u32x2*)(lds + L_TILE + tid * 8);
          st128(rs, W_Q + qs * 256 + (tid >> 4) * 16384 + n * 16, u32x4{t2[0], t2[1], e0, e1});
        }
        STAMP(15); TSA(7);
      }
      SCHED();
    }
    STAMP(3); TSB(0);
    const unsigned char* qg = ws + W_Q + (b_att * 1024 + kvh * 64) * 16;
    vo[0] = wpart + ((grp & 1) << AB[0]) + ((grp >> 1) << AB[1]) + n * 16;
    vo[1] = vo[0] + (1 << AB[2]); vo[2] = vo[0] + (1 << AB[3]); vo[3] = vo[0] + (1 << AB[2]) + (1 << AB[3]);
    TSB(1);
    PACE(32); VLD(0); WAITV(DK); VLD(1); WAITV(DK); PACE(34); VLD(2); WAITV(DK); VLD(3); WAITV(DK);
    PACE(36); VLD(4); WAITV(DK); VLD(5); WAITV(DK);
    if (wave == 1) dma_sc(qg, lane * 16, lds_base + L_B + 1024);
    PACE(38); VLD(6); WAITV(DK); VLD(7); WAITV(DK);
    PACE(40); VLD(8); WAITV(DV); VLD(9); WAITV(DV);
    if (wave == 1) dma_sc(qg, lane * 16, lds_base + L_B + 1024);
    PACE(42); VLD(10); WAITV(DV); VLD(11); WAITV(DV);
    TIE8(kb, 0); TIE8(kb, 8); TIE8(kb, 16); TIE8(kb, 24);
    PACE(44); VLD(12); VLD(13);
    u32x4 qf[2];
    {
      const u32x4* ql = (const u32x4*)(lds + L_B + 1024 + (((n >> 2) & 1) * 32 + grp * 8 + (n & 3) * 2) * 16);
      u32x4 g[2];
      _Pragma("nounroll") for (int sp = 0; sp < SPIN_CAP; ++sp) {
        g[0] = ql[0]; g[1] = ql[1];
        if (__all((tag_x(g[0], e0, e1) | tag_x(g[1], e0, e1)) == 0u)) break;
        if (wave == 1) { dma_sc(qg, lane * 16, lds_base + L_B + 1024); WAITV(0); } else nap(4);
      }
      asm volatile("" ::: "memory");
      g[0] = ql[0]; g[1] = ql[1];
      qf[0] = n < 8 ? u32x4{g[0][0], g[0][1], g[1][0], g[1][1]} : u32x4{0, 0, 0, 0};
      qf[1] = n < 8 ? u32x4{0, 0, 0, 0} : u32x4{g[0][0], g[0][1], g[1][0], g[1][1]};
    }
    STAMP(4); TSB(2);
    constexpr float SCALE = 0.088388347648318440f;
    float sc[4];
#pragma unroll
    for (int i = 0; i < 4; ++i) {
      float y[2];
#pragma unroll
      for (int t = 0; t < 2; ++t) {
        const int u = 8 * t + 2 * i;
        const f32x4 a = mfma(kb[u + 16], qf[1], mfma(kb[u], qf[0], f32x4{0.f, 0.f, 0.f, 0.f}));
        const f32x4 b = mfma(kb[u + 17], qf[1], mfma(kb[u + 1], qf[0], f32x4{0.f, 0.f, 0.f, 0.f}));
        const float own = (n & 2) ? ((n & 1) ? b[3] : a[2]) : ((n & 1) ? b[1] : a[0]), oth = (n & 2) ? ((n & 1) ? a[3] : b[2]) : ((n & 1) ? a[1] : b[0]);
        y[t] = own + dppf<0xB1>(oth);
      }
      sc[i] = ((n & 2) ? y[1] : y[0]) + dppf<0x4E>((n & 2) ? y[0] : y[1]);
      if (i == 1 || i == 3) { SCHED(); if (i == 1) VLD(14); else VLD(15); SCHED(); }
    }
#pragma unroll
    for (int i = 0; i < 4; ++i) sc[i] = rbf(rbf(sc[i]) * SCALE);
    float mx = fmaxf(fmaxf(sc[0], sc[1]), fmaxf(sc[2], sc[3]));
    mx = fmaxf(mx, dppf<0xB1>(mx)); mx = fmaxf(mx, dppf<0x4E>(mx)); mx = fmaxf(mx, dppf<0x128>(mx));
    mx = xmax32(xmax16(mx));
#pragma unroll
    for (int i = 0; i < 4; ++i) sc[i] = __expf(sc[i] - mx);
    float ls = (sc[0] + sc[1]) + (sc[2] + sc[3]);
    ls += dppf<0xB1>(ls); ls += dppf<0x4E>(ls); ls += dppf<0x128>(ls);
    ls = xsum32(xsum16(ls));
    float* stat = (float*)(lds + L_B);
    if (tid == 0) *(u32*)(lds + L_B + 512) = 0u;
    if (lane == 0 || lane == 4) *(f32x2*)(stat + (wave * 2 + (lane >> 2)) * 2) = f32x2{mx, ls};
    SCHED(); PACE(48); VLD(16); VLD(17); SCHED();
    TSB(3);
    lds_barrier();
    TSB(4);
    float M = -INFINITY, L = 0.f;
    {
      f32x2 t[8];
#pragma unroll
      for (int q = 0; q < 8; ++q) { t[q] = *(const f32x2*)(stat + (q * 2 + ((n >> 2) & 1)) * 2); M = fmaxf(M, t[q].x); }
#pragma unroll
      for (int q = 0; q < 8; ++q) L = fmaf(__expf(t[q].x - M), t[q].y, L);
    }
    if (tid < 8) st128(rs, W_ST + (w * 8 + lane) * 16, u32x4{__float_as_uint(M), __float_as_uint(L), e0, e1});
    SCHED();
#define VTR(G) do { u32x4 f_[8]; _Pragma("unroll") for (int t = 0; t < 8; ++t) _Pragma("unroll") for (int j = 0; j < 4; ++j) \
      f_[t][j] = (t & 1) ? phi(vb[(G) * 8 + 2 * j][t >> 1], vb[(G) * 8 + 2 * j + 1][t >> 1]) : plo(vb[(G) * 8 + 2 * j][t >> 1], vb[(G) * 8 + 2 * j + 1][t >> 1]); \
      _Pragma("unroll") for (int t = 0; t < 8; ++t) { vb[(G) * 8 + t] = f_[t]; asm volatile("" : "+v"(vb[(G) * 8 + t])); } SCHED(); } while (0)
    WAITV(DV);
    TIE8(vb, 0);
    VTR(0);
    if (gs >= 0) {
      const unsigned char* mb = ws + W_M + (8 + wave) * 1152;
      const unsigned ld_ = lds_base + L_W + wave * 2304;
      dma_sc(mb, lane * 16, ld_); dma_sc(mb + 9216, lane * 16, ld_ + 1024);
      if (lane < 16) dma_sc(mb + 1024, (lane >> 3) * 9216 + (lane & 7) * 16, ld_ + 2048);
    } else if (oj >= 0 && tid >= 504) {
      dma_sc(ws + W_M + op * 1152 + oj * 128, (lane & 7) * 16, lds_base + L_B + 3072 - 896);
      dma_sc((const char*)x + op * 1152 + oj * 128, (lane & 7) * 16, lds_base + L_B + 3200 - 896);
    }
    PACE(50); VLD(18); WAITV(DV); VLD(19); WAITV(DV);
    PACE(52); VLD(20); WAITV(5);
    TIE8(vb, 8);
    VTR(1);
    VLD(21); WAITV(DV);
    const int po = W_ST + ((w ^ 1) * 8 + (n & 4)) * 16;
    PACE(54);
    VLD(22); WAITV(DV); VLD(23); WAITV(DV);
    u32x4 ps;
    ald128sc(ps, ws, po);
    PACE(56);
    VLD(24); WAITV(DV); VLD(25); WAITV(DV);
    PACE(58);
    VLD(26); WAITV(DV); VLD(27); WAITV(DV);
    UNTIL(tp0 + 60 * pr_ / 4 - E60);
    VLD(28); VLD(29); WAITV(6);
    asm volatile("" : "+v"(ps));
    TIE8(vb, 16);
    UNTIL(tp0 + 62 * pr_ / 4 - E62);
    VLD(30); VLD(31);
    VTR(2);
    if (!__all(tag_ok(ps, e0, e1))) {
      int sp = 0;
      _Pragma("nounroll") do { nap(1); ps = ld128(rs, po); } while (!__all(tag_ok(ps, e0, e1)) && ++sp < SPIN_CAP);
    }
    float fac;
    {
      const float M2 = __uint_as_float(ps[0]), L2 = __uint_as_float(ps[1]);
      const float Mg = fmaxf(M, M2);
      const float Lg = __expf(M - Mg) * L + __expf(M2 - Mg) * L2;
      fac = __expf(mx - Mg) / Lg;
    }
    STAMP(1); TSB(5);
    u32x4 pf[4];
#pragma unroll
    for (int j = 0; j < 4; ++j) {
      const float p_ = sc[j] * fac;
      pf[0][j] = cvt2(p_, dppf<0xB1>(p_));
      pf[1][j] = dppu<0x102>(pf[0][j]); pf[2][j] = dppu<0x108>(pf[0][j]); pf[3][j] = dppu<0x10A>(pf[0][j]);
    }
    f32x4 oa[8];
#pragma unroll
    for (int t = 0; t < 8; ++t) oa[t] = f32x4{0.f, 0.f, 0.f, 0.f};
    SCHED();
#pragma unroll
    for (int G = 0; G < 2; ++G)
#pragma unroll
      for (int t = 0; t < 8; ++t) oa[t] = mfma(pf[G], vb[G * 8 + t], oa[t]);
    SCHED();
#pragma unroll
    for (int t = 0; t < 8; ++t) oa[t] = mfma(pf[2], vb[16 + t], oa[t]);
    SCHED();
    int t2_ = threadIdx.x; asm volatile("" : "+v"(t2_));
    const int tid = t2_, lane = tid & 63, n = lane & 15, grp = lane >> 4;
    int oo = W_O + ((kvh * 8 + b_att) * 2 + s_att) * 1024 + lane * 16;
    int mw_ = L_C + wave * 1024 + lane * 32, mr_ = L_C + (grp * 16 + (lane & 7) * 2 + (n >> 3)) * 16;
    asm volatile("" : "+v"(oo), "+v"(mw_), "+v"(mr_), "+v"(pf[3]));
    u32x4 mg[2]; u32 mh[2];
    const u32 dv = (lane >> 3) * (gs >= 0 ? 128 : 1152) + (((lane & 7) ^ (lane >> 4)) << 4);
    const char* db = gs >= 0 ? (const char*)(wd + (size_t)gs * 64 * D) + wave * 1024 : (const char*)(wo + (size_t)(op * 512 + wave * 64) * D + oj * 64);
    const int NU = min(max(112 * H1 + 78 * (H2 + H3) - 576, 0), 572), NS = (2 * (576 - NU) + 111) / 112;
#define HROW(i_) (((i_) < NU ? (const char*)wu + (size_t)(576 - NU) * 9216 : (const char*)wg - (size_t)NU * 9216) + (size_t)max(min(i_, NU + 575), 0) * 9216 + xcd * 1152)
    const char* ha;
    if (gs >= 0) {
#pragma unroll
      for (int i = 0; i < 2; ++i) { mg[i] = *(const u32x4*)(lds + L_W + wave * 2304 + i * 1024 + lane * 16); mh[i] = *(const u32*)(lds + L_W + wave * 2304 + 2048 + i * 128 + (lane >> 1) * 4); }
      lds_barrier();
#pragma unroll
      for (int j = 0; j < 4; ++j) dma(db + j * 8192, dv, lds_base + L_W + 73728 + (j * 8 + wave) * 1024);
      WAITV(6);
    } else if (oj >= 0) {
      BAR();
#pragma unroll
      for (int j = 0; j < NB; ++j) dma(db + j * 8 * 1152, dv ^ ((j & 1) * 64), lds_base + L_W + (wave * 8 + j) * 1024);
      asm volatile("s_waitcnt vmcnt(%0)" :: "n"(NB + 2) : "memory"); SCHED();
    } else {
      const int r_ = max(min(lane / 9, H1 - 1), 0), c_ = lane - (lane / 9) * 9;
      ha = HROW(r_ * 112 + idx * 8 + wave) + c_ * 128;
      if (lane >= H1 * 9) {
        const int k_ = min((idx * 8 + wave) * NS + min(lane - H1 * 9, NS - 1), 2 * (576 - NU) - 1), g0 = ((3 - (int)((uintptr_t)wu >> 7) - xcd * 9) & 7) + (k_ & 1) * 8;
        ha = (const char*)wu + (size_t)(k_ >> 1) * 9216 + (xcd * 9 + (g0 < 9 ? g0 : g0 - 8)) * 128;
      }
      asm volatile("" : "+v"(ha));
      WAITV(2);
    }
    u32x4 vf[8];
    asm volatile("s_nop 0 ; both branches above cover V up to the last two" : "+v"(vb[24]), "+v"(vb[25]), "+v"(vb[26]), "+v"(vb[27]), "+v"(vb[28]), "+v"(vb[29]));
#pragma unroll
    for (int t = 0; t < 8; ++t)
#pragma unroll
      for (int j = 0; j < 3; ++j) vf[t][j] = (t & 1) ? phi(vb[24 + 2 * j][t >> 1], vb[25 + 2 * j][t >> 1]) : plo(vb[24 + 2 * j][t >> 1], vb[25 + 2 * j][t >> 1]);
#pragma unroll
    for (int t = 0; t < 8; ++t) asm volatile("" : "+v"(vf[t][0]), "+v"(vf[t][1]), "+v"(vf[t][2]));
    if (gs >= 0) WAITV(4); else if (oj >= 0) WAITV(NB); else { WAITV(0); dma_keep64(ha, lds_base + L_B + 4096 + wave * 256); }
    asm volatile("s_nop 0 ; both branches above cover V" : "+v"(vb[30]), "+v"(vb[31]));
#pragma unroll
    for (int t = 0; t < 8; ++t) {
      vf[t][3] = (t & 1) ? phi(vb[30][t >> 1], vb[31][t >> 1]) : plo(vb[30][t >> 1], vb[31][t >> 1]);
      oa[t] = mfma(pf[3], vf[t], oa[t]);
    }
    STAMP(5); TSB(6);
    if (grp < 2) {
      *(f32x4*)(lds + mw_) = f32x4{oa[0][0], oa[1][0], oa[2][0], oa[3][0]};
      *(f32x4*)(lds + mw_ + 16) = f32x4{oa[4][0], oa[5][0], oa[6][0], oa[7][0]};
    }
    lds_barrier();
    if (wave == 0) {
      f32x4 m[8];
#pragma unroll
      for (int q = 0; q < 8; ++q) m[q] = *(const f32x4*)(lds + mr_ + q * 1024);
      SCHED();
      const f32x4 o = ((m[0] + m[1]) + (m[2] + m[3])) + ((m[4] + m[5]) + (m[6] + m[7]));
      st128(rs, oo, u32x4{sanf(o[0]), sanf(o[1]), sanf(o[2]), sanf(o[3])});
    }
    STAMP(6); TSB(7); SCHED();
  if (oj >= 0) {
    const u32 t6o_ = (u32)__builtin_amdgcn_s_memrealtime();
    int wa = L_W + (wave >> 2) * 32768 + (16 * (grp >> 1) + 8 * (n >> 3) + 4 * (grp & 1) + ((n >> 2) & 1)) * 128 + (((2 * (wave & 3) + ((lane >> 1) & 1)) ^ (4 * (n >> 3) + 2 * (grp & 1))) << 4) + (lane & 1) * 8;
    int iw = L_OI + wave * 1024 + (((lane >> 1) & 3) * 16 + ((4 * grp + 2 * (lane & 1) + (n >> 3)) ^ ((lane >> 1) & 1) ^ (lane & 4))) * 16;
    int ir = L_OI + (wave >> 2) * 4096 + (grp * 16 + (n ^ (grp & 1) ^ (4 * (grp >> 1)))) * 16, ix = (lane & 2) ? 0xeeee : 0x4444;
    int rw = L_OI + 8192 + (wave >> 2) * 2304 + grp * 576 + (wave & 3) * 64 + n * 4, rr = L_OI + 8192 + wave * 288 + ((n & 7) * 2 + (n >> 3)) * 16;
    int xo = W_XP + (wave * 9 + oj) * 2048 + (n >> 3) * 1024 + op * 128 + (n & 7) * 16;
    int ob = W_O + ((op * 2 + (wave >> 2)) * 8 + (lane >> 3)) * 2048 + (wave & 3) * 256 + (lane & 7) * 16;
    asm volatile("" : "+v"(rw), "+v"(rr), "+v"(xo), "+v"(ob), "+v"(iw), "+v"(ir), "+v"(wa), "+v"(ix));
    float xv[2], ga[2]; int xi, xq;
    if (wave == 7) {
      const int g = lane & 7;
      u32 mm[2];
#pragma unroll
      for (int i = 0; i < 2; ++i) {
        xv[i] = b2f(*(const u16*)(lds + L_B + 3200 + g * 16 + i * 8 + grp * 2));
        mm[i] = *(const u32*)(lds + L_B + 3072 + g * 16 + i * 8 + (grp >> 1) * 4);
      }
      _Pragma("nounroll") for (int sp = 0; sp < SPIN_CAP && !__all(mxu(mm[0], mm[1]) != 0xffffffffu); ++sp) {
        nap(4);
#pragma unroll
        for (int i = 0; i < 2; ++i) mm[i] = ld32(rs, W_M + op * 1152 + oj * 128 + g * 16 + i * 8 + (grp >> 1) * 4);
      }
      ga[0] = pick2(mm[0], grp); ga[1] = pick2(mm[1], grp);
      xi = W_XP + (op * 9 + oj) * 2048 + lane * 16; xq = W_X1 + ((lane >> 3) * 8 + op) * 1152 + oj * 128 + g * 16;
      asm volatile("" : "+v"(xv[0]), "+v"(xv[1]), "+v"(ga[0]), "+v"(ga[1]), "+v"(xi), "+v"(xq));
    }
    u32x4 a[4];
    UNTIL(t6o_ + OD);
#define OLD() do { _Pragma("unroll") for (int p = 0; p < 4; ++p) a[p] = ld128(rs, ob + (p >> 1) * 1024 + (p & 1) * 128); } while (0)
#define OOK() __all(mxu(mxu(mx4(a[0]), mx4(a[1])), mxu(mx4(a[2]), mx4(a[3]))) != 0xffffffffu)
    OLD();
    SCHED();
    WAITV(4); BAR();
    TSC(0);
    u32x8 bw[4];
    f32x4 acc = {0.f, 0.f, 0.f, 0.f};
#define WFRAG(s0_, s1_) do { _Pragma("unroll") for (int s = s0_; s < s1_; ++s) { \
      const u32x2 t0 = trd(lds, wa + s * 8192), t1 = trd(lds, (wa ^ 16) + 256 + s * 8192), t2 = trd(lds, wa + 4096 + s * 8192), t3 = trd(lds, (wa ^ 16) + 4352 + s * 8192); \
      bw[s] = u32x8{t0[0], t0[1], t1[0], t1[1], t2[0], t2[1], t3[0], t3[1]}; } } while (0)
    WFRAG(0, 4);
    asm volatile("" : "+v"(bw[0]), "+v"(bw[1]), "+v"(bw[2]), "+v"(bw[3]), "+v"(acc));
    asm volatile("" : "+v"(a[0]), "+v"(a[1]), "+v"(a[2]), "+v"(a[3]));
    if (!OOK()) {
      int sp = 0;
      _Pragma("nounroll") do { nap(2); OLD(); } while (!OOK() && ++sp < SPIN_CAP);
    }
    STAMP(7); TSC(1);
#define OS(p, j) (__uint_as_float(a[p][j]) + __uint_as_float(a[(p) + 2][j]))
    *(u32x4*)(lds + iw) = u32x4{cvt2(OS(0, 0), OS(0, 1)), cvt2(OS(0, 2), OS(0, 3)), cvt2(OS(1, 0), OS(1, 1)), cvt2(OS(1, 2), OS(1, 3))};
    lds_barrier();
    TSC(2);
    u32x4 of[4];
#pragma unroll
    for (int k = 0; k < 4; ++k) { of[k] = *(const u32x4*)(lds + ir + k * 1024); SCHED(); }
#pragma unroll
    for (int k = 0; k < 4; ++k) acc = smf(of[k], bw[k], acc, ix);
    const f32x2 e = smf_sum(acc);
    *(float*)(lds + rw) = e.x; *(float*)(lds + rw + 288) = e.y;
    lds_barrier();
    TSC(3);
    if (lane < 16) {
      const f32x4 r = *(const f32x4*)(lds + rr) + *(const f32x4*)(lds + rr + 2304);
      st128(rs, xo, u32x4{sanf(r[0]), sanf(r[1]), sanf(r[2]), sanf(r[3])});
    }
    TSC(4);
    if (wave == 7) {
      u32x4 xp[2];
      _Pragma("nounroll") for (int sp = 0; sp < SPIN_CAP; ++sp) {
        xp[0] = ld128(rs, xi); xp[1] = ld128(rs, xi + 1024);
        if (__all(mxu(mx4(xp[0]), mx4(xp[1])) != 0xffffffffu)) break;
        nap(1);
      }
      TSC(5);
      const u32x2 r0 = gather4(rbf(xv[0] + rbf(ga[0] * rbf(red8(asf4(xp[0])))))), r1 = gather4(rbf(xv[1] + rbf(ga[1] * rbf(red8(asf4(xp[1]))))));
      st128(rs, xq, u32x4{sanu(r0[0]), sanu(r0[1]), sanu(r1[0]), sanu(r1[1])});
      if (lane == 0) st128(rs, W_TS + (oj * 8 + op) * 128, u32x4{(u32)__builtin_amdgcn_s_memrealtime(), 0u, e0, e1});
      STAMPW(8); TSC(6);
    }
  }
  if (oj >= 0) STAMP(13);
  if (gs >= 0) {
    constexpr int L_H2 = L_B;
    constexpr int L_ACT = L_C + 4608;
    const int b = wave;
    u32x8 bw[9], wdf[5];
    f32x4 d[6];
    const u32 gv = (lane >> 3) * (FF * 2) + (((lane & 7) ^ ((wave & 1) * 4 + (lane >> 4))) << 4);
#define GDMA(j0, j1) do { _Pragma("unroll") for (int j = j0; j < j1; ++j) dma((const char*)(wg + gs * 64) + (size_t)(j * 64 + wave * 8) * (FF * 2), gv, lds_base + L_W + 73728 + (j * 8 + wave) * 1024); } while (0)
#define UDMA(j0, j1) do { _Pragma("unroll") for (int j = j0; j < j1; ++j) dma((const char*)(wu + gs * 64) + (size_t)(j * 64 + wave * 8) * (FF * 2), gv ^ 16u, lds_base + L_W + (j * 8 + wave) * 1024); } while (0)
    BAR(); SCHED();
    TSC(0);
    float sb[9], cb1[9];
    int ia[2], ra, wa, ix, yo[3], tw, tr_, xa, xb;
    u32x4 x8, fy; u32 x1;
    asm volatile("" : "=v"(fy));
    int fo = W_FY + min(lane, NPY / 2 - 1) * 16;
    {
#pragma unroll
      for (int j = 4; j < 9; ++j) dma(db + j * 8192, dv, lds_base + L_W + 73728 + (j * 8 + wave) * 1024);
      BAR();
      UDMA(0, UA);
    ia[0] = L_H2 + (lane >> 3) * 1024 + (((lane >> 1) & 3) * 16 + ((4 * (b >> 1) + 2 * (lane & 1) + (b & 1)) ^ ((lane >> 1) & 1) ^ (lane & 4))) * 16;
    ia[1] = L_H2 + 8192 + (grp * 16 + ((4 * (b >> 1) + 2 * (n >> 3) + (b & 1)) ^ (grp & 1) ^ (4 * (grp >> 1)))) * 16 + (lane & 7) * 2;
    ra = L_H2 + (grp * 16 + (n ^ (grp & 1) ^ (4 * (grp >> 1)))) * 16;
    wa = L_W + ((lane & 2) ? 0 : 73728) + (16 * (grp >> 1) + 8 * (n >> 3) + 4 * (grp & 1) + ((n >> 2) & 1)) * 128 + ((wave ^ (4 * (n >> 3) + 2 * (grp & 1)) ^ ((lane >> 1) & 1)) << 4) + (lane & 1) * 8;
    ix = (lane & 2) ? 0xeeee : 0x4444;
    tw = L_ACT + ((wave >> 1) * 16 + 4 * grp + (n & 2) + (n >> 3)) * 16 + ((n >> 2) & 1) * 8 + (wave & 1) * 4 + (n & 1) * 2; tr_ = L_ACT + lane * 16;
    yo[0] = W_Y + (((grp * 2) * 18 + wave * 2 + (n >> 3)) * NG + gs) * 128 + (n & 7) * 16;
    yo[1] = W_Y + ((grp * 2 * 18 + 16 + ((wave >> 1) & 1)) * NG + gs) * 128 + (wave & 1) * 64 + n * 4;
    yo[2] = yo[0] + 18 * NG * 128;
    xa = W_X1 + (xcd * 8 + b) * 1152 + lane * 16; xb = W_X1 + (xcd * 8 + b) * 1152 + 1024 + (lane >> 1) * 4;
    asm volatile("" : "+v"(ia[0]), "+v"(ia[1]), "+v"(ra), "+v"(wa), "+v"(ix), "+v"(tw), "+v"(tr_), "+v"(yo[0]), "+v"(yo[1]), "+v"(yo[2]), "+v"(xa), "+v"(xb), "+v"(fo));
#define MOK() __all(mxu(mxu(mx4(mg[0]), mx4(mg[1])), mxu(mh[0], mh[1])) != 0xffffffffu)
#define MLD(b_) do { _Pragma("unroll") for (int i = 0; i < 2; ++i) { mg[i] = ld128(rs, W_M + (8 + i * 8 + (b_)) * 1152 + lane * 16); mh[i] = ld32(rs, W_M + (8 + i * 8 + (b_)) * 1152 + 1024 + (lane >> 1) * 4); } } while (0)
#define MUNP() do { _Pragma("unroll") for (int i = 0; i < 4; ++i) { sb[2 * i] = lo16(mg[0][i]); sb[2 * i + 1] = hi16(mg[0][i]); \
                      cb1[2 * i] = rbf(1.f + lo16(mg[1][i])); cb1[2 * i + 1] = rbf(1.f + hi16(mg[1][i])); } \
                    sb[8] = pick2(mh[0], lane); cb1[8] = rbf(1.f + pick2(mh[1], lane)); \
                    _Pragma("unroll") for (int i = 0; i < 9; ++i) asm volatile("" : "+v"(sb[i]), "+v"(cb1[i])); } while (0)
      if (!MOK()) {
        int sp = 0;
        _Pragma("nounroll") do { nap(2); MLD(b); } while (!MOK() && ++sp < SPIN_CAP);
      }
      MUNP();
      WAITV(UA); BAR();
      {
        const int q = n >> 2, p = lane & 3, r = 16 * (grp >> 1) + 4 * (grp & 1) + q;
        const int a0 = L_W + 73728 + r * 1152 + (p & 1) * 8;
#pragma unroll
        for (int t = 0; t < 5; ++t) {
          const int a = t < 4 ? a0 + wave * 128 + (((t + (p >> 1) * 4) ^ (((r + wave) >> 1) & 3)) << 4) : a0 + 1024 + ((((wave & 3) * 2 + (p >> 1)) ^ ((r >> 1) & 3)) << 4);
          const u32x2 t0 = trd(lds, a), t1 = trd(lds, a + 9216), t2 = trd(lds, a + 36864), t3 = trd(lds, a + 36864 + 9216);
          wdf[t] = u32x8{t0[0], t0[1], t1[0], t1[1], t2[0], t2[1], t3[0], t3[1]};
        }
#pragma unroll
        for (int t = 0; t < 5; ++t) asm volatile("" : "+v"(wdf[t]));
      }
      TSC(1);
      UDMA(UA, 9);
      lds_barrier();
      GDMA(0, 9);
      WAITV(4); BAR();
      WFRAG(0, 5);
      asm volatile("" : "+v"(bw[0]), "+v"(bw[1]), "+v"(bw[2]), "+v"(bw[3]), "+v"(bw[4]));
#pragma unroll
      for (int t = 0; t < 6; ++t) { d[t] = f32x4{0.f, 0.f, 0.f, 0.f}; asm volatile("" : "+v"(d[t])); }
      TSC(2);
      WAITV(0);
      x8 = ld128(rs, xa); x1 = ld32(rs, xb);
      SCHED();
      BAR();
      WFRAG(5, 9);
      SCHED();
      STAMP(13);
    }
    {
      TSC(3);
#define XOK() __all(mxu(mx4(x8), x1) != 0xffffffffu)
      if (!XOK()) {
        int sp = 0;
        _Pragma("nounroll") do { nap(1); x8 = ld128(rs, xa); x1 = ld32(rs, xb); } while (!XOK() && ++sp < SPIN_CAP);
      }
      STAMP(9); TSC(4);
      if (wave == 0) fy = ld128(rs, fo);
      SCHED();
      u32x4 h8; u32 h1;
      ln9f(x8, pick2(x1, lane), cb1, sb, h8, h1);
      *(u32x4*)(lds + ia[0]) = h8;
      *(u16*)(lds + ia[1]) = (u16)h1;
      TSC(5);
      lds_barrier();
      u32x4 af[9];
#pragma unroll
      for (int s = 0; s < 9; ++s) { af[s] = *(const u32x4*)(lds + ra + s * 1024); SCHED(); }
      f32x4 acc = d[5];
#pragma unroll
      for (int s = 0; s < 9; ++s) acc = smf(af[s], bw[s], acc, ix);
      const f32x2 vv = smf_sum(acc);
      const int v0 = __float_as_int(vv.x), v1 = __float_as_int(vv.y);
      const float g = __int_as_float(__builtin_amdgcn_update_dpp(v0, v1, 0x128, 0xf, 0xc, false)), u = __int_as_float(__builtin_amdgcn_update_dpp(v1, v0, 0x128, 0xf, 0x3, false));
      const float gr = rbf(g);
      *(u16*)(lds + tw) = (u16)cvt2(rbf(gr * frcp(1.f + __expf(-gr))) * rbf(u), 0.f);
      SCHED();
      if (wave == 0 && !FOK(fy)) {
        int sp = 0;
        _Pragma("nounroll") do { nap(2); fy = ld128(rs, fo); } while (!FOK(fy) && ++sp < SPIN_CAP);
      }
    }
    lds_barrier();
    STAMP(10); TSC(6);
    {
      const u32x4 af = *(const u32x4*)(lds + tr_);
#pragma unroll
      for (int t = 0; t < 4; ++t) d[t] = smf(af, wdf[t], d[t], ix);
      SCHED();
      f32x2 e[5];
#pragma unroll
      for (int t = 0; t < 4; ++t) e[t] = smf_sum(d[t]);
#pragma unroll
      for (int j = 0; j < 2; ++j)
        st128(rs, yo[2 * j], u32x4{sanf(e[0][j]), sanf(e[1][j]), sanf(e[2][j]), sanf(e[3][j])});
      SCHED();
      if (wave < 4) {
        e[4] = smf_sum(smf(af, wdf[4], d[4], ix));
        __builtin_amdgcn_raw_buffer_store_b32(sanf(e[4][0]), rs, yo[1], 0, 16);
        __builtin_amdgcn_raw_buffer_store_b32(sanf(e[4][1]), rs, yo[1], 18 * NG * 128, 16);
      }
      TSC(7);
    }
  }
  {
  int t3_ = threadIdx.x; asm volatile("" : "+v"(t3_));
  const int tid = t3_, lane = tid & 63, n = lane & 15, grp = lane >> 4;
  const u32 t6_ = (u32)__builtin_amdgcn_s_memrealtime();
  if (idx >= 1 && idx < 14 && wave >= 2) {
    const int r_ = min(lane / 9, H2 - 1), c_ = lane - (lane / 9) * 9;
    ha = HROW(112 * H1 + ((wave - 2) * 13 + idx - 1) * H2 + r_) + c_ * 128;
    asm volatile("" : "+v"(ha));
    UNTIL(t6_ + HD2 + (wave - 2) * HS2);
    TSC(0);
    {
      const rsrc_t ry = __builtin_amdgcn_make_buffer_rsrc(ws + W_Y, 0, 144 * NG * 128, 0x00020000);
      const u32x4 ones = {0xffffffffu, 0xffffffffu, 0xffffffffu, 0xffffffffu};
      const int yp = (w - 8) * 13312 + (tid - 128) * 16;
      if (wave == 2) st128(ry, yp + 12288, ones);
      st128(ry, yp, ones); st128(ry, yp + 6144, ones);
      SCHED();
    }
    dma_keep64(ha, lds_base + L_B + 4096 + wave * 256);
    WAITV(1);
    if (lane == 0 && __hip_atomic_fetch_add((__attribute__((address_space(3))) u32*)((lds_t*)lds + L_B + 512), 1u, __ATOMIC_RELAXED, __HIP_MEMORY_SCOPE_WORKGROUP) == 5u)
      st64(rs, W_FY + (w - 8) * 8, u32x2{e0, e1});
    TSC(1);
    if (H3 > 0) {
      ha = HROW(112 * H1 + 78 * H2 + ((wave - 2) * 13 + idx - 1) * H3 + min(lane / 9, H3 - 1)) + c_ * 128;
      asm volatile("" : "+v"(ha));
      UNTIL(t6_ + HD3 + (wave - 2) * HS2);
      dma_keep64(ha, lds_base + L_B + 4096 + wave * 256);
    }
  }
  if (ml >= 0) {
    BAR();
    slab_dma(w_mod + ml * 64, NMOD * 2, lds_base + L_W, wave, lane);
    u32x4 wf[9], af[9];
    img_frags(lds, L_IMG, lane, af);
    asm volatile("s_waitcnt vmcnt(0)\n\ts_barrier" ::: "memory");
    slab_frags(lds, L_W, wave, lane, wf);
    const f32x4 s = half_sum(gemv9(wf, af));
    if (n < 8 && grp < 2) *(u32x2*)(lds + L_TILE + n * 128 + wave * 16 + grp * 8) = u32x2{cvt2(s[0], s[1]), cvt2(s[2], s[3])};
    lds_barrier();
    MPUB(ml);
  }

  if (idx >= 5 && idx < 23 && wave == 1) {
    const int b = xcd, jh = idx - 5, col = jh * 32 + (jh < 16 ? (lane & 7) + grp * 8 : (lane & 7) * 4 + grp);
    const rsrc_t rw_ = __builtin_amdgcn_make_buffer_rsrc((void*)w_r, 0, D * 2, 0x00020000);
    const u32x4 wr8 = __builtin_amdgcn_raw_buffer_load_b128(rw_, lane * 16, 0, 0);
    const float wr1 = b2f(__builtin_amdgcn_raw_buffer_load_b16(rw_, 1024 + lane * 2, 0, 0));
    u32x4 mg[2]; u32 mh[2];
    _Pragma("nounroll") for (int sp = 0; sp < SPIN_CAP; ++sp) { MLD(b); if (MOK()) break; nap(4); }
    float sb[9], cb1[9];
    MUNP();
    const int xa = W_X1 + (xcd * 8 + b) * 1152 + lane * 16, xb = W_X1 + (xcd * 8 + b) * 1152 + 1024 + (lane >> 1) * 4;
    u32x4 x8; u32 x1;
    _Pragma("nounroll") for (int sp = 0; sp < SPIN_CAP; ++sp) { x8 = ld128(rs, xa); x1 = ld32(rs, xb); if (XOK()) break; nap(1); }
    const u32 tx_ = (u32)__builtin_amdgcn_s_memrealtime();
    const int go = W_M + (24 + b) * 1152 + (col >> 1) * 4, fo = W_FY + min(lane, NPY / 2 - 1) * 16;
    u32 gq = ld32(rs, go); u32x4 fy = ld128(rs, fo);
    const float v8 = pick2(x1, lane);
    u32x4 h8; u32 h1;
    ln9(x8, v8, cb1, sb, h8, h1);
    unsigned char* row = lds + L_C;
    *(u32x4*)(row + lane * 16) = x8; *(u32x4*)(row + 1152 + lane * 16) = h8;
    *(u16*)(row + 1024 + lane * 2) = (u16)(__float_as_uint(v8) >> 16); *(u16*)(row + 2176 + lane * 2) = (u16)h1;
    float r = lo16(h1) * wr1;
#pragma unroll
    for (int i = 0; i < 4; ++i) r = fmaf(hi16(h8[i]), hi16(wr8[i]), fmaf(lo16(h8[i]), lo16(wr8[i]), r));
    r = rbf(wsum(r));
    float rsel = rbf(1.f / (1.f + __expf(-r)));
    float x1v = b2f(*(const u16*)(row + col * 2)), h2 = b2f(*(const u16*)(row + 1152 + col * 2));
    _Pragma("nounroll") for (int sp = 0; sp < SPIN_CAP && !(__all(gq != 0xffffffffu) && FOK(fy)); ++sp) { nap(2); gq = ld32(rs, go); fy = ld128(rs, fo); }
    float gbv = pick2(gq, col);
    u32 tgo;
    {
      const u32x4 tq = ld128(rs, W_TS + lane * 128);
      int d_ = tag_ok(tq, e0, e1) ? (int)(tq[0] - tx_) : 0;
      d_ = max(d_, (int)dppu<0xB1>(d_)); d_ = max(d_, (int)dppu<0x4E>(d_)); d_ = max(d_, (int)dppu<0x141>(d_)); d_ = max(d_, (int)dppu<0x140>(d_));
      d_ = max(max(__builtin_amdgcn_readlane(d_, 0), __builtin_amdgcn_readlane(d_, 16)), max(__builtin_amdgcn_readlane(d_, 32), __builtin_amdgcn_readlane(d_, 48)));
      tgo = tx_ + min(d_ + TGO, TGO - 15);
    }
    int yi = W_Y + (b * 18 + jh) * NG * 128 + lane * 16, oo = (b * D + col) * 2;
    asm volatile("" : "+v"(rsel), "+v"(x1v), "+v"(h2), "+v"(gbv), "+v"(yi), "+v"(oo));
    TSC(5);
    UNTIL(tgo);
    float z;
    _Pragma("nounroll") for (int sp = 0; sp < SPIN_CAP; ++sp) {
      u32x4 y[9];
#pragma unroll
      for (int i = 0; i < 9; ++i) y[i] = ld128(rs, yi + i * 1024);
      z = red8((((asf4(y[0]) + asf4(y[1])) + (asf4(y[2]) + asf4(y[3]))) + ((asf4(y[4]) + asf4(y[5])) + (asf4(y[6]) + asf4(y[7])))) + asf4(y[8]));
      if (__all(z == z)) break;
      u32 m = mx4(y[8]);
#pragma unroll
      for (int i = 0; i < 8; ++i) m = mxu(m, mx4(y[i]));
      if (__all(m != 0xffffffffu)) break;
      nap(1);
    }
    STAMPW(11); TSC(6);
    const float y2 = rbf(rbf(z) * rsel);
    const float res = x1v + rbf(gbv * rbf(h2 + y2));
    if (!(lane & 8)) __builtin_amdgcn_raw_buffer_store_b16((u16)(cvt2(res, 0.f) & 0xffffu), __builtin_amdgcn_make_buffer_rsrc(out, 0, 8 * D * 2, 0x00020000), oo, 0, 0);
    STAMPW(12); TSC(7);
  }
  TSOUT();
  }
  }
}

extern "C" void kernel_launch(void* const* d_in, const int* in_sizes, int n_in, void* d_out, int out_size, void* d_ws, size_t ws_size,
                              hipStream_t stream) {
  fused_kernel<<<NWG, NT, 0, stream>>>((const u16*)d_in[0], (const u16*)d_in[1], (const u16*)d_in[2], (const u16*)d_in[3], (const u16*)d_in[4],
                                       (const u16*)d_in[6], (const u16*)d_in[7], (const u16*)d_in[8], (const u16*)d_in[9], (const u16*)d_in[10],
                                       (const u16*)d_in[11], (unsigned char*)d_ws, (u16*)d_out);
}
```

```cpp
#include <hip/hip_runtime.h>
#include <hip/hip_bf16.h>
#include <cstdint>

typedef unsigned u32;
typedef unsigned short u16;
typedef u32 u32x4 __attribute__((ext_vector_type(4)));
typedef u32 u32x2 __attribute__((ext_vector_type(2)));
typedef float f32x4 __attribute__((ext_vector_type(4)));
typedef float f32x2 __attribute__((ext_vector_type(2)));
typedef short s16x4 __attribute__((ext_vector_type(4)));
typedef __bf16 bf16x8 __attribute__((ext_vector_type(8)));
typedef __bf16 bf16x2 __attribute__((ext_vector_type(2)));
typedef __attribute__((address_space(3))) unsigned char lds_t;

constexpr int D = 576, SK = 8192, WIN = 2048, FF = 4608, NQ = 4096, NMOD = 6 * D;
constexpr int NWG = 256, NT = 512, NG = 72;
constexpr int SPIN_CAP = 1 << 15;

constexpr int W_O = 0;
constexpr int W_XP = W_O + 262144;
constexpr int W_X1 = W_XP + 147456;
constexpr int W_M = W_X1 + 73728;
constexpr int W_MS = W_M + 36864;
constexpr int W_Y = W_MS;
constexpr int W_FL = W_Y + 144 * NG * 128;
constexpr int W_FY = W_FL + 1024;
constexpr int W_H = W_FY + 1024;
constexpr int W_Q = W_H + 36864;
constexpr int W_ST = W_Q + 131072;
constexpr int W_TS = W_ST + 32768;
constexpr int W_END = W_TS + 72 * 128;
constexpr int PZ = (W_MS / 16 + NG - 1) / NG;
constexpr int NPY = 106, PY = (144 * NG * 8 + NPY - 1) / NPY;
static_assert(PZ <= NT && PY <= 3 * 384, "poison split");

constexpr int L_W = 0;
constexpr int L_IMG = 73728;
constexpr int L_TILE = L_IMG + 9216;
constexpr int L_OI = 65536;
constexpr int L_B = 147456;
constexpr int L_C = 155648;
constexpr int LDS_BYTES = 163840;

#define STAMP(i) do {} while (0)
#define STAMPW(i) do {} while (0)

#define SCHED() __builtin_amdgcn_sched_barrier(0)
#define UNTIL(t) do { _Pragma("nounroll") while ((int)((u32)__builtin_amdgcn_s_memrealtime() - (t)) < 0) __builtin_amdgcn_s_sleep(1); } while (0)
#define BAR() asm volatile("s_barrier" ::: "memory")
#define TS(i) do {} while (0)
#define TSOUT() do {} while (0)
#define TSA(i) do {} while (0)
#define TSB(i) do {} while (0)
#define TSC(i) do {} while (0)
#define WAITV_(n) do { asm volatile("s_waitcnt vmcnt(" #n ")" ::: "memory"); SCHED(); } while (0)
#define WAITV(n) WAITV_(n)
#define DK 5
#define DV DK
#define PR 117
#define PR2 113
#define TADJ -10
#define SLOWJ 6
#define TGO 200

__device__ __forceinline__ float lo16(u32 u) { return __uint_as_float(u << 16); }
__device__ __forceinline__ float hi16(u32 u) { return __uint_as_float(u & 0xffff0000u); }
__device__ __forceinline__ float b2f(u16 u) { return __uint_as_float((u32)u << 16); }
__device__ __forceinline__ u32 cvt2(float a, float b) { return __builtin_bit_cast(u32, __builtin_convertvector(f32x2{a, b}, bf16x2)); }
__device__ __forceinline__ float rbf(float v) { return __uint_as_float(cvt2(0.f, v)); }
__device__ __forceinline__ u32 sanf(float f) { u32 v = __float_as_uint(f); return v < 0xfffffffeu ? v : 0xfffffffeu; }
__device__ __forceinline__ u32 sanu(u32 v) { return v < 0xfffffffeu ? v : 0xfffffffeu; }
__device__ __forceinline__ f32x4 asf4(u32x4 v) { return __builtin_bit_cast(f32x4, v); }
__device__ __forceinline__ f32x2 up2(u32 u) { return f32x2{lo16(u), hi16(u)}; }
__device__ __forceinline__ f32x2 rbf2(f32x2 v) { return f32x2{rbf(v.x), rbf(v.y)}; }
__device__ __forceinline__ void lds_barrier() { asm volatile("s_waitcnt lgkmcnt(0)\n\ts_barrier" ::: "memory"); }
__device__ __forceinline__ f32x4 mfma(u32x4 a, u32x4 b, f32x4 c) {
  return __builtin_amdgcn_mfma_f32_16x16x32_bf16(__builtin_bit_cast(bf16x8, a), __builtin_bit_cast(bf16x8, b), c, 0, 0, 0);
}
__device__ __forceinline__ u32 plo(u32 a, u32 b) { return __builtin_amdgcn_perm(b, a, 0x05040100u); }
__device__ __forceinline__ u32 phi(u32 a, u32 b) { return __builtin_amdgcn_perm(b, a, 0x07060302u); }

extern "C" __device__ __attribute__((const)) unsigned long long llvm_dispatch_id() __asm("llvm.amdgcn.dispatch.id");
typedef __amdgpu_buffer_rsrc_t rsrc_t;
__device__ __forceinline__ u32x4 ld128(rsrc_t rs, int off) { return __builtin_amdgcn_raw_buffer_load_b128(rs, off, 0, 16); }
__device__ __forceinline__ u32 ld32(rsrc_t rs, int off) { return __builtin_amdgcn_raw_buffer_load_b32(rs, off, 0, 16); }
__device__ __forceinline__ void st128(rsrc_t rs, int off, u32x4 v) { __builtin_amdgcn_raw_buffer_store_b128(v, rs, off, 0, 16); }
__device__ __forceinline__ void st64(rsrc_t rs, int off, u32x2 v) { __builtin_amdgcn_raw_buffer_store_b64(v, rs, off, 0, 16); }
__device__ __forceinline__ void dma(const void* gbase, u32 voff, unsigned lds_dst) {
  unsigned keep;
  asm volatile("s_mov_b32 %0, m0\n\ts_mov_b32 m0, %3\n\ts_nop 2\n\tglobal_load_lds_dwordx4 %1, %2 nt\n\ts_mov_b32 m0, %0"
               : "=&s"(keep) : "v"(voff), "s"(gbase), "s"(lds_dst) : "memory");
}
__device__ __forceinline__ void dma_keep(const void* gbase, u32 voff, unsigned lds_dst) {
  unsigned keep;
  asm volatile("s_mov_b32 %0, m0\n\ts_mov_b32 m0, %3\n\ts_nop 2\n\tglobal_load_lds_dword %1, %2\n\ts_mov_b32 m0, %0"
               : "=&s"(keep) : "v"(voff), "s"(gbase), "s"(lds_dst) : "memory");
}
__device__ __forceinline__ void dma_sc(const void* gbase, u32 voff, unsigned lds_dst) {
  unsigned keep;
  asm volatile("s_mov_b32 %0, m0\n\ts_mov_b32 m0, %3\n\ts_nop 2\n\tglobal_load_lds_dwordx4 %1, %2 sc1\n\ts_mov_b32 m0, %0"
               : "=&s"(keep) : "v"(voff), "s"(gbase), "s"(lds_dst) : "memory");
}
__device__ __forceinline__ void ald128(u32x4& d, const void* gbase, u32 voff) { unsigned long long t; asm volatile("s_mov_b64 %1, %3\n\tglobal_load_dwordx4 %0, %2, %1" : "=v"(d), "=&s"(t) : "v"(voff), "s"(gbase) : "memory"); }
__device__ __forceinline__ void ald64(u32x2& d, const void* gbase, u32 voff) { unsigned long long t; asm volatile("s_mov_b64 %1, %3\n\tglobal_load_dwordx2 %0, %2, %1" : "=v"(d), "=&s"(t) : "v"(voff), "s"(gbase) : "memory"); }
__device__ __forceinline__ void ald128sc(u32x4& d, const void* gbase, u32 voff) { unsigned long long t; asm volatile("s_mov_b64 %1, %3\n\tglobal_load_dwordx4 %0, %2, %1 sc1" : "=v"(d), "=&s"(t) : "v"(voff), "s"(gbase) : "memory"); }
__device__ __forceinline__ u32x2 trd(const unsigned char* lds, int off) {
  return __builtin_bit_cast(u32x2, __builtin_amdgcn_ds_read_tr16_b64_v4i16((__attribute__((address_space(3))) s16x4*)((lds_t*)lds + off)));
}
#define nap(n) do { __builtin_amdgcn_s_sleep(n); asm volatile("" ::: "memory"); } while (0)
__device__ __forceinline__ float frcp(float v) { return __builtin_amdgcn_rcpf(v); }
__device__ __forceinline__ float fdiv(float a, float d, float r) { const float q = a * r; return fmaf(fmaf(-q, d, a), r, q); }
__device__ __forceinline__ float silu(float v) { const float d = 1.f + __expf(-v); const float r = frcp(d); return d < 3.0e38f ? fdiv(v, d, r) : v * r; }
template <int CTRL> __device__ __forceinline__ float dppf(float x) {
  return __builtin_bit_cast(float, __builtin_amdgcn_mov_dpp(__builtin_bit_cast(int, x), CTRL, 0xf, 0xf, true));
}
template <int CTRL> __device__ __forceinline__ u32 dppu(u32 x) { return (u32)__builtin_amdgcn_mov_dpp((int)x, CTRL, 0xf, 0xf, true); }
__device__ __forceinline__ u32 mxu(u32 a, u32 b) { return a > b ? a : b; }
__device__ __forceinline__ u32 mx4(u32x4 v) { return mxu(mxu(v[0], v[1]), mxu(v[2], v[3])); }
__device__ __forceinline__ float rl(float v, int l) { return __builtin_bit_cast(float, __builtin_amdgcn_readlane(__builtin_bit_cast(int, v), l)); }
__device__ __forceinline__ float wsum(float v) {
  v += dppf<0xB1>(v); v += dppf<0x4E>(v); v += dppf<0x141>(v); v += dppf<0x140>(v);
  return (rl(v, 0) + rl(v, 16)) + (rl(v, 32) + rl(v, 48));
}
__device__ __forceinline__ float up32(float v) { const u32 u = __float_as_uint(v); return __uint_as_float(__builtin_amdgcn_permlane32_swap(u, u, false, false)[1]); }
__device__ __forceinline__ float xsum32(float v) { const u32 u = __float_as_uint(v); const auto r = __builtin_amdgcn_permlane32_swap(u, u, false, false); return __uint_as_float(r[0]) + __uint_as_float(r[1]); }
__device__ __forceinline__ float xsum16(float v) { const u32 u = __float_as_uint(v); const auto r = __builtin_amdgcn_permlane16_swap(u, u, false, false); return __uint_as_float(r[0]) + __uint_as_float(r[1]); }
__device__ __forceinline__ float xmax32(float v) { const u32 u = __float_as_uint(v); const auto r = __builtin_amdgcn_permlane32_swap(u, u, false, false); return fmaxf(__uint_as_float(r[0]), __uint_as_float(r[1])); }
__device__ __forceinline__ float xmax16(float v) { const u32 u = __float_as_uint(v); const auto r = __builtin_amdgcn_permlane16_swap(u, u, false, false); return fmaxf(__uint_as_float(r[0]), __uint_as_float(r[1])); }
__device__ __forceinline__ float red8(f32x4 v) {
  const auto a = __builtin_amdgcn_permlane32_swap(__float_as_uint(v[0]), __float_as_uint(v[2]), false, false);
  const auto b = __builtin_amdgcn_permlane32_swap(__float_as_uint(v[1]), __float_as_uint(v[3]), false, false);
  const float w0 = __uint_as_float(a[0]) + __uint_as_float(a[1]), w1 = __uint_as_float(b[0]) + __uint_as_float(b[1]);
  const auto c = __builtin_amdgcn_permlane16_swap(__float_as_uint(w0), __float_as_uint(w1), false, false);
  const float z = __uint_as_float(c[0]) + __uint_as_float(c[1]);
  return z + dppf<0x128>(z);
}
__device__ __forceinline__ u32x2 gather4(float z) {
  const u32 u = __float_as_uint(z);
  const auto a = __builtin_amdgcn_permlane16_swap(u, u, false, false);
  const u32 pk = phi(a[0], a[1]);
  const auto b = __builtin_amdgcn_permlane32_swap(pk, pk, false, false);
  return u32x2{b[0], b[1]};
}
__device__ __forceinline__ f32x4 half_sum(f32x4 d) {
  f32x4 r;
#pragma unroll
  for (int j = 0; j < 4; ++j) r[j] = d[j] + dppf<0x108>(up32(d[j]));
  return r;
}
__device__ __forceinline__ float half_sum1(f32x4 d, bool lo) {
  const auto a = __builtin_amdgcn_permlane32_swap(__float_as_uint(d[0]), __float_as_uint(d[2]), false, false);
  const auto b = __builtin_amdgcn_permlane32_swap(__float_as_uint(d[1]), __float_as_uint(d[3]), false, false);
  return __uint_as_float(lo ? a[0] : b[1]) + dppf<0x128>(__uint_as_float(lo ? b[0] : a[1]));
}
__device__ __forceinline__ void unpack8(u32x4 u, float* f) {
#pragma unroll
  for (int j = 0; j < 4; ++j) { f[2 * j] = lo16(u[j]); f[2 * j + 1] = hi16(u[j]); }
}
__device__ __forceinline__ void ln_row(const float (&v)[16], bool has2, float& mean, float& den) {
  float s = 0.f;
#pragma unroll
  for (int i = 0; i < 8; ++i) s += v[i];
  if (has2) {
#pragma unroll
    for (int i = 8; i < 16; ++i) s += v[i];
  }
  s = wsum(s);
  const float mu = s * (1.f / D);
  mean = rbf(mu);
  float q = 0.f;
#pragma unroll
  for (int i = 0; i < 8; ++i) { const float t = v[i] - mu; q = fmaf(t, t, q); }
  if (has2) {
#pragma unroll
    for (int i = 8; i < 16; ++i) { const float t = v[i] - mu; q = fmaf(t, t, q); }
  }
  q = wsum(q);
  const float var = rbf(q * (1.f / D));
  den = rbf(sqrtf(rbf(var + 1e-5f)));
}
__device__ __forceinline__ float pick2(u32 t, int e) { return (e & 1) ? hi16(t) : lo16(t); }
__device__ __forceinline__ void ln9(u32x4 x8, float v8, const float (&cb1)[9], const float (&sb)[9], u32x4& h8, u32& h1) {
  f32x2 v[4];
#pragma unroll
  for (int i = 0; i < 4; ++i) v[i] = up2(x8[i]);
  const f32x2 s2 = (v[0] + v[1]) + (v[2] + v[3]);
  const float mu = wsum((s2.x + s2.y) + v8) * (1.f / D);
  const f32x2 mu2 = {mu, mu};
  const float t8 = v8 - mu;
  f32x2 q = {t8 * t8, 0.f};
#pragma unroll
  for (int i = 0; i < 4; ++i) { const f32x2 t = v[i] - mu2; q = __builtin_elementwise_fma(t, t, q); }
  const float mean = rbf(mu);
  f32x2 d[4];
#pragma unroll
  for (int i = 0; i < 4; ++i) d[i] = rbf2(v[i] - f32x2{mean, mean});
  const float d8 = rbf(v8 - mean);
  const float var = rbf(wsum(q.x + q.y) * (1.f / D));
  const float rden = frcp(rbf(__builtin_amdgcn_sqrtf(rbf(var + 1e-5f))));
#pragma unroll
  for (int i = 0; i < 4; ++i) {
    const f32x2 t = rbf2(rbf2(d[i] * f32x2{rden, rden}) * f32x2{cb1[2 * i], cb1[2 * i + 1]}) + f32x2{sb[2 * i], sb[2 * i + 1]};
    h8[i] = cvt2(t.x, t.y);
  }
  h1 = cvt2(rbf(rbf(d8 * rden) * cb1[8]) + sb[8], 0.f);
}
template <int J0 = 0, int J1 = 9>
__device__ __forceinline__ void slab_dma(const u16* base, int stride_b, unsigned lds_dst, int wave, int lane) {
  const u32 v0 = (lane >> 3) * stride_b + (((lane & 7) ^ ((wave & 1) * 4 + (lane >> 4))) << 4), v1 = v0 ^ 32u;
  const u32 v4 = wave >= 4 ? v1 : v0;
  const char* gb = (const char*)base + (size_t)wave * 8 * stride_b;
#pragma unroll
  for (int j = J0; j < J1; ++j) dma(gb + (size_t)j * 64 * stride_b, j < 4 ? v0 : j == 4 ? v4 : v1, lds_dst + (j * 8 + wave) * 1024);
}
__device__ __forceinline__ void slab_frags(const unsigned char* lds, int base, int wave, int lane, u32x4 (&wf)[9]) {
  const int g = lane >> 4, q = (lane & 15) >> 2, p = lane & 3, hf = p >> 1;
  const int a0 = base + (hf * 288 + 8 * g + q) * 128 + ((wave ^ ((g & 1) * 4 + (q >> 1)) ^ (hf * 2)) << 4) + (p & 1) * 8, a1 = (a0 ^ 32) + 512;
#pragma unroll
  for (int s = 0; s < 9; ++s) {
    const u32x2 lo = trd(lds, a0 + s * 4096), hi = trd(lds, a1 + s * 4096);
    wf[s] = u32x4{lo[0], lo[1], hi[0], hi[1]};
  }
}
__device__ __forceinline__ void img_frags(const unsigned char* lds, int img, int lane, u32x4 (&af)[9]) {
#pragma unroll
  for (int s = 0; s < 9; ++s) af[s] = *(const u32x4*)(lds + img + (s * 64 + lane) * 16);
}
__device__ __forceinline__ f32x4 gemv9(const u32x4 (&wf)[9], const u32x4 (&af)[9]) {
  f32x4 acc[3];
  SCHED();
#pragma unroll
  for (int s = 0; s < 9; ++s) acc[s % 3] = mfma(wf[s], af[s], s < 3 ? f32x4{0.f, 0.f, 0.f, 0.f} : acc[s % 3]);
  return (acc[0] + acc[1]) + acc[2];
}
__device__ __forceinline__ int img_off(int b, int k8) {
  const int hf = k8 >= 36 ? 1 : 0, kk = k8 - hf * 36;
  return ((kk >> 2) * 64 + (kk & 3) * 16 + hf * 8 + b) * 16;
}
__device__ __forceinline__ u32 tag_x(u32x4 v, u32 e0, u32 e1) { return (v[2] ^ e0) | (v[3] ^ e1); }
__device__ __forceinline__ bool tag_ok(u32x4 v, u32 e0, u32 e1) { return tag_x(v, e0, e1) == 0u; }

constexpr int CODE_BYTES = 30592;

__global__ void __launch_bounds__(NT, 2) fused_kernel(const u16* __restrict__ x, const u16* __restrict__ kc, const u16* __restrict__ vc,
                                                      const u16* __restrict__ wq, const u16* __restrict__ wo, const u16* __restrict__ w_r,
                                                      const u16* __restrict__ wg, const u16* __restrict__ wu, const u16* __restrict__ wd,
                                                      const u16* __restrict__ c, const u16* __restrict__ w_mod, unsigned char* ws, u16* out) {
  __shared__ __attribute__((aligned(16))) unsigned char lds[LDS_BYTES];
  int w = blockIdx.x;
  const int tid = threadIdx.x, lane = tid & 63, wave = __builtin_amdgcn_readfirstlane(tid >> 6);
  const int n = lane & 15, grp = lane >> 4;
  const size_t pairbase = ((size_t)(w >> 1) * SK + (SK - WIN)) * 256 + (w & 1) * 16384;
  const int wpart = (wave & 3) * 4096 + (wave >> 2) * 262144;
  const int ko = wpart + (n >> 2) * 256 + grp * 64 + (n & 3) * 16;
  const char* kp = (const char*)kc + pairbase;
  u32x4 kb[32], vb[32];
#define KLD(u) do { unsigned long long t_; asm volatile("s_mov_b64 %1, %3\n\tglobal_load_dwordx4 %0, %2, %1 offset:%4 nt" : "=v"(kb[u]), "=&s"(t_) : "v"(ko), "s"(kp + ((u) >> 2) * 32768), "i"(((u) & 3) * 1024) : "memory"); } while (0)
  unsigned long long tst;
  asm volatile("s_memrealtime %0" : "=s"(tst) : "s"(kp));
  KLD(0); KLD(1); KLD(2); KLD(3);
  asm volatile("s_waitcnt lgkmcnt(0)" : "+s"(w), "+s"(ws), "+s"(tst));
  const u32 tstart = (u32)tst;
  const int xcd = w & 7, idx = w >> 3;
  const rsrc_t rs = __builtin_amdgcn_make_buffer_rsrc(ws, 0, W_END, 0x00020000);
  const unsigned lds_base = (unsigned)(uintptr_t)(lds_t*)lds;
  const unsigned long long epoch = (llvm_dispatch_id() + 1ull) * 0x9E3779B97F4A7C15ull;
  const u32 e0 = (u32)epoch, e1 = (u32)(epoch >> 32);
  unsigned long long pc0; asm volatile("s_getpc_b64 %0" : "=s"(pc0));
  asm volatile("" : "+s"(vc), "+s"(w_r), "+s"(wu), "+s"(wd), "+s"(out), "+s"(x), "+s"(w_mod), "+s"(wg), "+s"(wq), "+s"(wo));

  const int mw = idx < 6 ? idx * 8 + xcd : -1;
  const int md = mw < 18 ? mw : -1;
  const int me = mw >= 18 ? mw : -1;
  const int ml = mw >= 0 && mw < 6 ? 48 + mw : -1;
  const int qs = idx >= 6 && idx < 14 ? xcd * 8 + idx - 6 : -1;
  const int oj = idx >= 14 && idx < 23 ? idx - 14 : -1, op = xcd;
  const int gs = idx >= 23 ? xcd * 9 + idx - 23 : -1;

  const int s_att = w & 1, kvh = (w >> 1) & 15, b_att = w >> 5;
  {
    const char* vp = (const char*)vc + pairbase;
    const int vo = wpart + lane * 16;
#define VLD(u) do { unsigned long long t_; asm volatile("s_mov_b64 %1, %3\n\tglobal_load_dwordx4 %0, %2, %1 offset:%4 nt" : "=v"(vb[u]), "=&s"(t_) : "v"(vo), "s"(vp + ((u) >> 2) * 32768), "i"(((u) & 3) * 1024) : "memory"); } while (0)
#define TIE8(a, o) asm volatile("" : "+v"(a[o]), "+v"(a[(o) + 1]), "+v"(a[(o) + 2]), "+v"(a[(o) + 3]), "+v"(a[(o) + 4]), "+v"(a[(o) + 5]), "+v"(a[(o) + 6]), "+v"(a[(o) + 7]))
    const int pr_ = gs >= 0 ? PR2 : PR;
    const u32 tp0 = tstart + 9 * pr_ / 4 - TADJ;
    if (idx == 23 && tid == 0) st128(rs, W_FY + 896 + xcd * 16, u32x4{tstart, 0u, e0, e1});
#define PACE(i) do { _Pragma("nounroll") while ((int)((u32)__builtin_amdgcn_s_memrealtime() - tp0) < (i) * pr_ / 4) __builtin_amdgcn_s_sleep(2); SCHED(); } while (0)
#define REP4(M, u, W) do { M(u); W; M((u) + 1); W; M((u) + 2); W; M((u) + 3); W; } while (0)
    static_assert(DK <= 7 && DV <= 7, "the hand-placed waits assume it");
#define POISON() do { if (gs >= 0) { const int g_ = gs * PZ + tid; \
      if (tid < PZ && g_ < W_MS / 16) st128(rs, g_ * 16, u32x4{0xffffffffu, 0xffffffffu, 0xffffffffu, 0xffffffffu}); } SCHED(); } while (0)
#define PFLAG() do { if (gs >= 0 && tid == 448) st64(rs, W_FL + gs * 8, u32x2{e0, e1}); SCHED(); } while (0)
#define FOK(a) __all(((a[0] ^ e0) | (a[1] ^ e1) | tag_x(a, e0, e1)) == 0u)
#define FREQ() do { if (wave == 1) dma_sc(ws + W_FL, min(lane, NG / 2 - 1) * 16, lds_base + L_B + 2048); } while (0)
#define FCHK() do { if (wave == 1 && !FOK((*(const u32x4*)(lds + L_B + 2048 + lane * 16)))) { \
      _Pragma("nounroll") for (int sp = 0; sp < SPIN_CAP; ++sp) { nap(4); const u32x4 a_ = ld128(rs, W_FL + min(lane, NG / 2 - 1) * 16); if (FOK(a_)) break; } } SCHED(); } while (0)
#define SILU_IMAGE(c0, c1) do { float f[8]; unpack8(c0, f); u32x4 o; \
        _Pragma("unroll") for (int j = 0; j < 4; ++j) o[j] = cvt2(silu(f[2 * j]), silu(f[2 * j + 1])); \
        const int b_ = tid / 72, ch_ = tid - b_ * 72; \
        *(u32x4*)(lds + L_IMG + img_off(b_, ch_)) = o; \
        if (wave == 0) { unpack8(c1, f); \
          _Pragma("unroll") for (int j = 0; j < 4; ++j) o[j] = cvt2(silu(f[2 * j]), silu(f[2 * j + 1])); \
          *(u32x4*)(lds + L_IMG + img_off(7, 8 + lane)) = o; } } while (0)
    if (md >= 0) {
      u32x4 c0, c1, x0, x1; u32x2 xc;
      TSA(0);
      int ho = W_H + img_off(n & 7, (md >= 9 ? md - 9 : md) * 8 + wave) * 2 + (grp & 1) * 16;
      ald128(c0, c, tid * 16);
      ald128(c1, c, (512 + lane) * 16);
      ald128(x0, x, wave * 1152 + lane * 16);
      ald128(x1, x, wave * 1152 + 1024 + (lane & 7) * 16);
      ald64(xc, x, (n & 7) * 1152 + ((md >= 9 ? md - 9 : 0) * 64 + wave * 8 + (grp & 1) * 4) * 2);
      BAR();
      float* st = (float*)(lds + L_TILE + 1024);
      if (wave < 4) {
        slab_dma(w_mod + md * 64, NMOD * 2, lds_base + L_W, wave, lane);
        asm volatile("s_waitcnt vmcnt(9)" ::: "memory");
      } else asm volatile("s_waitcnt vmcnt(0)" ::: "memory");
      asm volatile("s_nop 0 ; both branches above cover V" : "+v"(c0), "+v"(c1), "+v"(x0), "+v"(x1), "+v"(xc) :: "memory");
      TSA(1);
      {
        SILU_IMAGE(c0, c1);
        float v[16]; unpack8(x0, v); unpack8(x1, v + 8);
        float mean, den; ln_row(v, lane < 8, mean, den);
        if (lane == 0) { st[wave] = mean; st[8 + wave] = den; }
      }
      TSA(2);
      if (wave >= 4) slab_dma(w_mod + md * 64, NMOD * 2, lds_base + L_W, wave, lane);
      TSA(3);
      lds_barrier();
      REP4(KLD, 4, );
      u32x4 wf[9], af[9];
      img_frags(lds, L_IMG, lane, af);
      const float mean = st[n & 7], rd = frcp(st[8 + (n & 7)]);
      float ln[4] = {rbf(rbf(lo16(xc[0]) - mean) * rd), rbf(rbf(hi16(xc[0]) - mean) * rd), rbf(rbf(lo16(xc[1]) - mean) * rd), rbf(rbf(hi16(xc[1]) - mean) * rd)};
#pragma unroll
      for (int s = 0; s < 9; ++s) asm volatile("" : "+v"(af[s]));
      asm volatile("" : "+v"(ln[0]), "+v"(ln[1]), "+v"(ln[2]), "+v"(ln[3]), "+v"(ho));
      WAITV(4); BAR(); SCHED();
      TSA(5);
      slab_frags(lds, L_W, wave, lane, wf);
      const f32x4 s = half_sum(gemv9(wf, af));
      STAMP(14); TSA(6);
      f32x4 r = s;
      if (md >= 9) {
#pragma unroll
        for (int j = 0; j < 4; ++j) r[j] = ln[j] * rbf(1.f + rbf(s[j]));
      }
      if (n < 8 && grp < 2) st128(rs, ho + (md >= 9 ? 18432 : 0), u32x4{cvt2(r[0], r[1]), cvt2(r[2], r[3]), e0, e1});
      SCHED();
      STAMP(15); TSA(7);
      BAR();
      KLD(8); KLD(9); WAITV(DK); KLD(10); WAITV(DK);
      KLD(11); WAITV(DK); REP4(KLD, 12, WAITV(DK)); REP4(KLD, 16, WAITV(DK));
      FREQ(); REP4(KLD, 20, WAITV(DK)); PACE(24); REP4(KLD, 24, WAITV(DK)); PACE(28); KLD(28); WAITV(DK); KLD(29);
      FCHK();
    } else {
      constexpr int L_LAND = 98304;
      const unsigned land = lds_base + L_LAND + wave * 6144;
      const unsigned char* lp = lds + L_LAND + wave * 6144 + lane * 16;
      const int sl_ = (3 - (int)((uintptr_t)wq >> 7)) & 7, sm_ = (63 - (int)((uintptr_t)wq >> 7)) & 63, sg_ = (3 - (int)((uintptr_t)wg >> 7)) & 7;
      const bool slowq = (qs >= 0 && ((qs & 7) == sl_ || qs == sm_)) || (gs >= 0 && (gs & 7) == sg_);
      if (oj >= 0 && wave < 2) {
        if (wave == 0 || (sm_ >> 3) == xcd) dma_keep((const char*)wq + (size_t)oj * 64 * 8192 + (wave == 0 ? xcd * 8 + sl_ : sm_) * 128, lane * 8192, land);
      } else if (oj >= 0 && wave < 4) {
        const int g0 = ((sg_ - xcd * 9) & 7) + (wave - 2) * 8;
        if (g0 < 9) dma_keep((const char*)wg + (size_t)oj * 64 * 9216 + (xcd * 9 + g0) * 128, lane * 9216, land);
      }
      POISON();
      if (oj >= 0 && oj < 8 && wave == 4)
        dma_keep((const void*)pc0, min((oj & 3) * 8192 + lane * 128, CODE_BYTES - 128), land);
      const u32 v0 = (lane >> 3) * 1152 + (((lane & 7) ^ ((lane >> 4) & 1)) << 4);
      const char* gb = (const char*)(wo + (size_t)(op * 512 + wave * 64) * D + oj * 64);
      if (me >= 0) { dma_sc(c, tid * 16, land); dma_sc(c, (512 + lane) * 16, land + 1024); }
      const bool fs = qs >= 0 || me >= 0;
      const u16* sbase = qs >= 0 ? wq + qs * 64 : gs >= 0 ? wg + gs * 64 : w_mod + me * 64;
      const int sstr = qs >= 0 ? NQ * 2 : gs >= 0 ? FF * 2 : NMOD * 2;
      const unsigned sdst = lds_base + L_W + (gs >= 0 ? 73728 : 0);
#define WK(j) do { if (oj >= 0) { if ((j) < 8) dma(gb + (j) * 8 * 1152, v0 ^ (((j) & 1) * 32 + ((j) >> 2) * 64), lds_base + L_W + (wave * 8 + (j)) * 1024); } \
                   else if (slowq) { if ((j) == SLOWJ) slab_dma(sbase, sstr, sdst, wave, lane); } \
                   else slab_dma<j, (j) + 1>(sbase, sstr, sdst, wave, lane); \
                   KLD((j) + 4); WAITV(DK); } while (0)
      WK(0); WK(1); WK(2); WK(3);
      WK(4); WK(5);
      static_assert(DK <= 5, "six younger requests cover the poison");
      if (me >= 0) { const u32x4 c0 = *(const u32x4*)lp, c1 = *(const u32x4*)(lp + 1024); SILU_IMAGE(c0, c1); }
      BAR();
      PFLAG();
      WK(6); WK(7); WK(8);
      PACE(13); KLD(13); WAITV(DK); KLD(14); WAITV(DK); KLD(15); WAITV(DK);
      const int hg0 = (wave * 144 + lane) * 16, hg2 = (wave * 144 + 128 + n) * 16;
#define HREQ() do { dma_sc(ws + W_H, hg0, land); dma_sc(ws + W_H + 18432, hg0, land + 1024); dma_sc(ws + W_H + 1024, hg0, land + 2048); \
                    dma_sc(ws + W_H + 18432 + 1024, hg0, land + 3072); dma_sc(ws + W_H, hg2, land + 4096); dma_sc(ws + W_H + 18432, hg2, land + 5120); } while (0)
#define HGET() do { _Pragma("unroll") for (int i = 0; i < 6; ++i) hq[i] = *(const u32x4*)(lp + i * 1024); } while (0)
#define HQOK() __all((tag_x(hq[0], e0, e1) | tag_x(hq[1], e0, e1) | tag_x(hq[2], e0, e1) | tag_x(hq[3], e0, e1) | tag_x(hq[4], e0, e1) | tag_x(hq[5], e0, e1)) == 0u)
      PACE(16); KLD(16); WAITV(DK); KLD(17); WAITV(DK); PACE(18); KLD(18); WAITV(DK); KLD(19); WAITV(DK);
      TSA(4);
      if (qs >= 0) HREQ();
      PACE(20); FREQ(); KLD(20); WAITV(DK); KLD(21); WAITV(DK); PACE(22); KLD(22); WAITV(DK); KLD(23); WAITV(DK);
      if (qs >= 0) WAITV(4);
      TSA(5);
      PACE(24); KLD(24); KLD(25);
      if (qs >= 0) {
        u32x4 hq[6]; HGET();
        _Pragma("nounroll") for (int sp = 0; sp < SPIN_CAP && !HQOK(); ++sp) { nap(16); HREQ(); WAITV(0); HGET(); }
        STAMP(14); TSA(6);
#define HSUM(a, b) cvt2(lo16(a) + lo16(b), hi16(a) + hi16(b))
        *(u32x2*)(lds + L_IMG + hg0 / 2) = u32x2{HSUM(hq[0][0], hq[1][0]), HSUM(hq[0][1], hq[1][1])};
        *(u32x2*)(lds + L_IMG + 512 + hg0 / 2) = u32x2{HSUM(hq[2][0], hq[3][0]), HSUM(hq[2][1], hq[3][1])};
        if (lane < 16) *(u32x2*)(lds + L_IMG + hg2 / 2) = u32x2{HSUM(hq[4][0], hq[5][0]), HSUM(hq[4][1], hq[5][1])};
      } else WAITV(DK);
      KLD(26); KLD(27);
      if (fs) {
        u32x4 wf[9], af[9];
        lds_barrier();
        slab_frags(lds, L_W, wave, lane, wf);
        img_frags(lds, L_IMG, lane, af);
        const f32x4 s = half_sum(gemv9(wf, af));
        if (n < 8 && grp < 2) *(u32x2*)(lds + L_TILE + n * 128 + wave * 16 + grp * 8) = u32x2{cvt2(s[0], s[1]), cvt2(s[2], s[3])};
      } else WAITV(DK);
      PACE(28); KLD(28); KLD(29);
      WAITV(9); FCHK();
#define MPUB(m_) do { if (wave == 0) { const u32x4 t_ = *(const u32x4*)(lds + L_TILE + lane * 16); const int ch_ = ((m_) - 18) / 9, sl_2 = (m_) - 18 - ch_ * 9; \
          st128(rs, W_M + (ch_ * 8 + (lane >> 3)) * 1152 + sl_2 * 128 + (lane & 7) * 16, u32x4{sanu(t_[0]), sanu(t_[1]), sanu(t_[2]), sanu(t_[3])}); } } while (0)
      if (fs) {
        lds_barrier();
        if (qs < 0) MPUB(me);
        else if (wave < 2) {
          const u32x2 t2 = *(const u32x2*)(lds + L_TILE + tid * 8);
          st128(rs, W_Q + qs * 256 + (tid >> 4) * 16384 + n * 16, u32x4{t2[0], t2[1], e0, e1});
        }
        STAMP(15); TSA(7);
      }
      SCHED();
    }
    STAMP(3); TSB(0);
    STAMP(2);
    const unsigned char* qg = ws + W_Q + (b_att * 1024 + kvh * 64) * 16;
    PACE(30); KLD(30); WAITV(DK); KLD(31); WAITV(DK);
    TSB(1);
    PACE(32); VLD(0); WAITV(DK); VLD(1); WAITV(DK); PACE(34); VLD(2); WAITV(DK); VLD(3); WAITV(DK);
    PACE(36); VLD(4); WAITV(DK); VLD(5); WAITV(DK);
    if (wave == 1) dma_sc(qg, lane * 16, lds_base + L_B + 1024);
    PACE(38); VLD(6); WAITV(DK); VLD(7); WAITV(DK);
    PACE(40); VLD(8); WAITV(DV); VLD(9); WAITV(DV);
    if (wave == 1) dma_sc(qg, lane * 16, lds_base + L_B + 1024);
    PACE(42); VLD(10); WAITV(DV); VLD(11); WAITV(DV);
    TIE8(kb, 0); TIE8(kb, 8); TIE8(kb, 16); TIE8(kb, 24);
    PACE(44); VLD(12); VLD(13);
    u32x4 qf[2];
    {
      const u32x4* ql = (const u32x4*)(lds + L_B + 1024 + (((n >> 2) & 1) * 32 + grp * 8 + (n & 3) * 2) * 16);
      u32x4 g[2];
      _Pragma("nounroll") for (int sp = 0; sp < SPIN_CAP; ++sp) {
        g[0] = ql[0]; g[1] = ql[1];
        if (__all((tag_x(g[0], e0, e1) | tag_x(g[1], e0, e1)) == 0u)) break;
        if (wave == 1) { dma_sc(qg, lane * 16, lds_base + L_B + 1024); WAITV(0); } else nap(4);
      }
      asm volatile("" ::: "memory");
      g[0] = ql[0]; g[1] = ql[1];
      qf[0] = n < 8 ? u32x4{g[0][0], g[0][1], g[1][0], g[1][1]} : u32x4{0, 0, 0, 0};
      qf[1] = n < 8 ? u32x4{0, 0, 0, 0} : u32x4{g[0][0], g[0][1], g[1][0], g[1][1]};
    }
    STAMP(4); TSB(2);
    constexpr float SCALE = 0.088388347648318440f;
    float sc[4];
#pragma unroll
    for (int i = 0; i < 4; ++i) {
      float y[2];
#pragma unroll
      for (int t = 0; t < 2; ++t) {
        const int u = 8 * t + 2 * i;
        const f32x4 a = mfma(kb[u + 16], qf[1], mfma(kb[u], qf[0], f32x4{0.f, 0.f, 0.f, 0.f}));
        const f32x4 b = mfma(kb[u + 17], qf[1], mfma(kb[u + 1], qf[0], f32x4{0.f, 0.f, 0.f, 0.f}));
        const float own = (n & 2) ? ((n & 1) ? b[3] : a[2]) : ((n & 1) ? b[1] : a[0]), oth = (n & 2) ? ((n & 1) ? a[3] : b[2]) : ((n & 1) ? a[1] : b[0]);
        y[t] = own + dppf<0xB1>(oth);
      }
      sc[i] = ((n & 2) ? y[1] : y[0]) + dppf<0x4E>((n & 2) ? y[0] : y[1]);
      if (i == 1 || i == 3) { SCHED(); if (i == 1) VLD(14); else VLD(15); SCHED(); }
    }
#pragma unroll
    for (int i = 0; i < 4; ++i) sc[i] = rbf(rbf(sc[i]) * SCALE);
    float mx = fmaxf(fmaxf(sc[0], sc[1]), fmaxf(sc[2], sc[3]));
    mx = fmaxf(mx, dppf<0xB1>(mx)); mx = fmaxf(mx, dppf<0x4E>(mx)); mx = fmaxf(mx, dppf<0x128>(mx));
    mx = xmax32(xmax16(mx));
#pragma unroll
    for (int i = 0; i < 4; ++i) sc[i] = __expf(sc[i] - mx);
    float ls = (sc[0] + sc[1]) + (sc[2] + sc[3]);
    ls += dppf<0xB1>(ls); ls += dppf<0x4E>(ls); ls += dppf<0x128>(ls);
    ls = xsum32(xsum16(ls));
    float* stat = (float*)(lds + L_B);
    if (tid == 0) *(u32*)(lds + L_B + 512) = 0u;
    if (lane == 0 || lane == 4) *(f32x2*)(stat + (wave * 2 + (lane >> 2)) * 2) = f32x2{mx, ls};
    SCHED(); PACE(48); VLD(16); VLD(17); SCHED();
    TSB(3);
    lds_barrier();
    TSB(4);
    float M = -INFINITY, L = 0.f;
    {
      f32x2 t[8];
#pragma unroll
      for (int q = 0; q < 8; ++q) { t[q] = *(const f32x2*)(stat + (q * 2 + ((n >> 2) & 1)) * 2); M = fmaxf(M, t[q].x); }
#pragma unroll
      for (int q = 0; q < 8; ++q) L = fmaf(__expf(t[q].x - M), t[q].y, L);
    }
    if (tid < 8) st128(rs, W_ST + (w * 8 + lane) * 16, u32x4{__float_as_uint(M), __float_as_uint(L), e0, e1});
    SCHED();
#define VTR(G) do { u32x4 f_[8]; _Pragma("unroll") for (int t = 0; t < 8; ++t) _Pragma("unroll") for (int j = 0; j < 4; ++j) \
      f_[t][j] = (t & 1) ? phi(vb[(G) * 8 + 2 * j][t >> 1], vb[(G) * 8 + 2 * j + 1][t >> 1]) : plo(vb[(G) * 8 + 2 * j][t >> 1], vb[(G) * 8 + 2 * j + 1][t >> 1]); \
      _Pragma("unroll") for (int t = 0; t < 8; ++t) { vb[(G) * 8 + t] = f_[t]; asm volatile("" : "+v"(vb[(G) * 8 + t])); } SCHED(); } while (0)
    WAITV(DV);
    TIE8(vb, 0);
    VTR(0);
    if (gs >= 0) {
      const unsigned char* mb = ws + W_M + (8 + wave) * 1152;
      const unsigned ld_ = lds_base + L_W + wave * 2304;
      dma_sc(mb, lane * 16, ld_); dma_sc(mb + 9216, lane * 16, ld_ + 1024);
      if (lane < 16) dma_sc(mb + 1024, (lane >> 3) * 9216 + (lane & 7) * 16, ld_ + 2048);
    }
    PACE(50); VLD(18); WAITV(DV); VLD(19); WAITV(DV);
    PACE(52); VLD(20); WAITV(5);
    TIE8(vb, 8);
    VTR(1);
    VLD(21); WAITV(DV);
    const int po = W_ST + ((w ^ 1) * 8 + (n & 4)) * 16;
    PACE(54);
    VLD(22); WAITV(DV); VLD(23); WAITV(DV);
    u32x4 ps;
    ald128sc(ps, ws, po);
    PACE(56);
    VLD(24); WAITV(DV); VLD(25); WAITV(DV);
    PACE(58);
    VLD(26); WAITV(DV); VLD(27); WAITV(DV);
    VLD(28); VLD(29); WAITV(6);
    asm volatile("" : "+v"(ps));
    TIE8(vb, 16);
    VLD(30); VLD(31);
    VTR(2);
    if (!__all(tag_ok(ps, e0, e1))) {
      int sp = 0;
      _Pragma("nounroll") do { nap(1); ps = ld128(rs, po); } while (!__all(tag_ok(ps, e0, e1)) && ++sp < SPIN_CAP);
    }
    float fac;
    {
      const float M2 = __uint_as_float(ps[0]), L2 = __uint_as_float(ps[1]);
      const float Mg = fmaxf(M, M2);
      const float Lg = __expf(M - Mg) * L + __expf(M2 - Mg) * L2;
      fac = __expf(mx - Mg) / Lg;
    }
    STAMP(1); TSB(5);
    u32x4 pf[4];
#pragma unroll
    for (int j = 0; j < 4; ++j) {
      const float p_ = sc[j] * fac;
      pf[0][j] = cvt2(p_, dppf<0xB1>(p_));
      pf[1][j] = dppu<0x102>(pf[0][j]); pf[2][j] = dppu<0x108>(pf[0][j]); pf[3][j] = dppu<0x10A>(pf[0][j]);
    }
    f32x4 oa[8];
#pragma unroll
    for (int t = 0; t < 8; ++t) oa[t] = f32x4{0.f, 0.f, 0.f, 0.f};
    SCHED();
#pragma unroll
    for (int G = 0; G < 2; ++G)
#pragma unroll
      for (int t = 0; t < 8; ++t) oa[t] = mfma(pf[G], vb[G * 8 + t], oa[t]);
    SCHED();
#pragma unroll
    for (int t = 0; t < 8; ++t) oa[t] = mfma(pf[2], vb[16 + t], oa[t]);
    SCHED();
    int t2_ = threadIdx.x; asm volatile("" : "+v"(t2_));
    const int tid = t2_, lane = tid & 63, n = lane & 15, grp = lane >> 4;
    int oo = W_O + ((kvh * 8 + b_att) * 2 + s_att) * 1024 + lane * 16;
    int mw_ = L_C + wave * 1024 + lane * 32, mr_ = L_C + (grp * 16 + (lane & 7) * 2 + (n >> 3)) * 16;
    asm volatile("" : "+v"(oo), "+v"(mw_), "+v"(mr_), "+v"(pf[3]));
    const bool late = gs >= 0;
    u32x4 bg[9], mg[2]; u32 mh[2];
    const u32 dv = (lane >> 3) * 128 + (((lane & 7) ^ ((lane >> 4) & 3)) << 4);
    const char* db = (const char*)(wd + (size_t)gs * 64 * D) + wave * 1024;
    if (late) {
      slab_frags(lds, L_W + 73728, wave, lane, bg);
#pragma unroll
      for (int i = 0; i < 2; ++i) { mg[i] = *(const u32x4*)(lds + L_W + wave * 2304 + i * 1024 + lane * 16); mh[i] = *(const u32*)(lds + L_W + wave * 2304 + 2048 + i * 128 + (lane >> 1) * 4); }
      lds_barrier();
#pragma unroll
      for (int j = 0; j < 4; ++j) dma(db + j * 8192, dv, lds_base + L_W + 73728 + (j * 8 + wave) * 1024);
      WAITV(6);
    } else {
      if (oj >= 0) {
        const int q = n >> 2, p = lane & 3, hf = p >> 1;
        const int a0 = L_W + (hf * 32 + 8 * grp + q) * 128 + (((q >> 1) ^ ((grp & 1) * 2) ^ (hf * 4) ^ wave) << 4) + (p & 1) * 8;
#pragma unroll
        for (int k = 0; k < 8; ++k) {
          const u32x2 lo = trd(lds, a0 + k * 8192), hi = trd(lds, a0 + k * 8192 + 512);
          bg[k] = u32x4{lo[0], lo[1], hi[0], hi[1]};
        }
#pragma unroll
        for (int k = 0; k < 8; ++k) asm volatile("" : "+v"(bg[k]));
      }
      WAITV(2);
    }
    u32x4 vf[8];
    asm volatile("s_nop 0 ; both branches above cover V up to the last two" : "+v"(vb[24]), "+v"(vb[25]), "+v"(vb[26]), "+v"(vb[27]), "+v"(vb[28]), "+v"(vb[29]));
#pragma unroll
    for (int t = 0; t < 8; ++t)
#pragma unroll
      for (int j = 0; j < 3; ++j) vf[t][j] = (t & 1) ? phi(vb[24 + 2 * j][t >> 1], vb[25 + 2 * j][t >> 1]) : plo(vb[24 + 2 * j][t >> 1], vb[25 + 2 * j][t >> 1]);
#pragma unroll
    for (int t = 0; t < 8; ++t) asm volatile("" : "+v"(vf[t][0]), "+v"(vf[t][1]), "+v"(vf[t][2]));
    if (late) WAITV(4); else WAITV(0);
    asm volatile("s_nop 0 ; both branches above cover V" : "+v"(vb[30]), "+v"(vb[31]));
#pragma unroll
    for (int t = 0; t < 8; ++t) {
      vf[t][3] = (t & 1) ? phi(vb[30][t >> 1], vb[31][t >> 1]) : plo(vb[30][t >> 1], vb[31][t >> 1]);
      oa[t] = mfma(pf[3], vf[t], oa[t]);
    }
    STAMP(5); TSB(6);
    if (grp < 2) {
      *(f32x4*)(lds + mw_) = f32x4{oa[0][0], oa[1][0], oa[2][0], oa[3][0]};
      *(f32x4*)(lds + mw_ + 16) = f32x4{oa[4][0], oa[5][0], oa[6][0], oa[7][0]};
    }
    lds_barrier();
    if (wave == 0) {
      f32x4 m[8];
#pragma unroll
      for (int q = 0; q < 8; ++q) m[q] = *(const f32x4*)(lds + mr_ + q * 1024);
      SCHED();
      const f32x4 o = ((m[0] + m[1]) + (m[2] + m[3])) + ((m[4] + m[5]) + (m[6] + m[7]));
      st128(rs, oo, u32x4{sanf(o[0]), sanf(o[1]), sanf(o[2]), sanf(o[3])});
    }
    STAMP(6); TSB(7); SCHED();
  if (oj >= 0) {
    int iw = L_OI + wave * 1024 + ((lane & 3) * 16 + ((lane >> 2) & 1) * 8 + ((lane >> 3) ^ (lane & 7))) * 16, ir = L_OI + (lane ^ ((n >> 3) * 4 + grp)) * 16;
    int rw = L_OI + 8192 + (n & 7) * 272 + wave * 32 + (grp & 1) * 16 + (grp >> 1) * 8 + (n >> 3) * 4, rr = L_OI + 8192 + wave * 272 + ((n & 7) * 2 + (n >> 3)) * 16;
    int xo = W_XP + (wave * 9 + oj) * 2048 + (n >> 3) * 1024 + op * 128 + (n & 7) * 16;
    int ob = W_O + ((op * 2 + (wave >> 2)) * 8 + (lane >> 3)) * 2048 + (wave & 3) * 256 + (lane & 7) * 16;
    asm volatile("" : "+v"(rw), "+v"(rr), "+v"(xo), "+v"(ob), "+v"(iw), "+v"(ir));
    TSC(0);
    nap(4);
    u32x4 a[4];
    _Pragma("nounroll") for (int sp = 0; sp < SPIN_CAP; ++sp) {
#pragma unroll
      for (int p = 0; p < 4; ++p) a[p] = ld128(rs, ob + (p >> 1) * 1024 + (p & 1) * 128);
      if (__all(mxu(mxu(mx4(a[0]), mx4(a[1])), mxu(mx4(a[2]), mx4(a[3]))) != 0xffffffffu)) break;
      nap(2);
    }
    STAMP(7); TSC(1);
#define OS(p, j) (__uint_as_float(a[p][j]) + __uint_as_float(a[(p) + 2][j]))
    *(u32x4*)(lds + iw) = u32x4{cvt2(OS(0, 0), OS(0, 1)), cvt2(OS(0, 2), OS(0, 3)), cvt2(OS(1, 0), OS(1, 1)), cvt2(OS(1, 2), OS(1, 3))};
    lds_barrier();
    TSC(2);
    u32x4 of[8];
#pragma unroll
    for (int k = 0; k < 8; ++k) { of[k] = *(const u32x4*)(lds + ir + k * 1024); SCHED(); }
    f32x4 acc = {0.f, 0.f, 0.f, 0.f};
#pragma unroll
    for (int k = 0; k < 8; ++k) acc = mfma(bg[k], of[k], acc);
    *(u32*)(lds + rw) = sanf(half_sum1(acc, n < 8));
    lds_barrier();
    TSC(3);
    if (lane < 16) st128(rs, xo, *(const u32x4*)(lds + rr));
    TSC(4);
  }
  if (oj >= 0) STAMP(13);
  if (gs >= 0) {
    constexpr int L_H2 = L_B;
    constexpr int L_ACT = L_C + 4608;
    const int b = wave;
    u32x4 bu[9], wdf[2][5];
    BAR(); SCHED();
    TSC(0);
    float sb[9], cb1[9];
    int ia[2], ra[4], yo[3], tw, tr_, xa, xb;
    u32x4 x8, fy = {e0, e1, e0, e1}; u32 x1;
    const int fo = W_FY + min(lane, NPY / 2 - 1) * 16;
    {
#pragma unroll
      for (int j = 4; j < 9; ++j) dma(db + j * 8192, dv, lds_base + L_W + 73728 + (j * 8 + wave) * 1024);
      BAR();
      slab_dma(wu + gs * 64, FF * 2, lds_base + L_W, wave, lane);
#pragma unroll
    for (int i = 0; i < 2; ++i) {
      const int k = i == 0 ? lane * 8 : 512 + lane, hf = k >= 288 ? 1 : 0, kk = k - hf * 288, s = kk >> 5, g = (kk >> 3) & 3;
      ia[i] = L_H2 + (s * 64 + g * 16 + ((hf * 8 + b) ^ ((s & 3) * 4 + g))) * 16 + (kk & 7) * 2;
    }
#pragma unroll
    for (int i = 0; i < 4; ++i) ra[i] = L_H2 + (grp * 16 + (n ^ (i * 4 + grp))) * 16;
    tw = L_ACT + (n & 7) * 144 + (wave >> 1) * 32 + (grp & 1) * 16 + (wave & 1) * 8 + (grp >> 1) * 4 + (n >> 3) * 2; tr_ = L_ACT + ((n >> 2) * 2 + (n & 1)) * 144 + grp * 16;
    yo[0] = W_Y + (((grp * 2) * 18 + wave * 2 + (n >> 3)) * NG + gs) * 128 + (n & 7) * 16;
    yo[1] = W_Y + ((grp * 2 * 18 + 16 + ((wave >> 1) & 1)) * NG + gs) * 128 + (wave & 1) * 64 + n * 4;
    yo[2] = yo[0] + 18 * NG * 128;
    xa = W_X1 + (xcd * 8 + b) * 1152 + lane * 16; xb = W_X1 + (xcd * 8 + b) * 1152 + 1024 + (lane >> 1) * 4;
    asm volatile("" : "+v"(ia[0]), "+v"(ia[1]), "+v"(ra[0]), "+v"(ra[1]), "+v"(ra[2]), "+v"(ra[3]), "+v"(tw), "+v"(tr_), "+v"(yo[0]), "+v"(yo[1]), "+v"(yo[2]), "+v"(xa), "+v"(xb));
#define MOK() __all(mxu(mxu(mx4(mg[0]), mx4(mg[1])), mxu(mh[0], mh[1])) != 0xffffffffu)
#define MLD(b_) do { _Pragma("unroll") for (int i = 0; i < 2; ++i) { mg[i] = ld128(rs, W_M + (8 + i * 8 + (b_)) * 1152 + lane * 16); mh[i] = ld32(rs, W_M + (8 + i * 8 + (b_)) * 1152 + 1024 + (lane >> 1) * 4); } } while (0)
#define MUNP() do { _Pragma("unroll") for (int i = 0; i < 4; ++i) { sb[2 * i] = lo16(mg[0][i]); sb[2 * i + 1] = hi16(mg[0][i]); \
                      cb1[2 * i] = rbf(1.f + lo16(mg[1][i])); cb1[2 * i + 1] = rbf(1.f + hi16(mg[1][i])); } \
                    sb[8] = pick2(mh[0], lane); cb1[8] = rbf(1.f + pick2(mh[1], lane)); \
                    _Pragma("unroll") for (int i = 0; i < 9; ++i) asm volatile("" : "+v"(sb[i]), "+v"(cb1[i])); } while (0)
      if (!MOK()) {
        int sp = 0;
        _Pragma("nounroll") do { nap(2); MLD(b); } while (!MOK() && ++sp < SPIN_CAP);
      }
      MUNP();
#pragma unroll
      for (int s = 0; s < 9; ++s) asm volatile("" : "+v"(bg[s]));
      asm volatile("s_waitcnt vmcnt(9)\n\ts_barrier" ::: "memory");
      {
        const int q = n >> 2, p = lane & 3, r = 16 * (grp >> 1) + 4 * (grp & 1) + q;
        const int a0 = L_W + 73728 + r * 1152 + (p & 1) * 8;
#pragma unroll
        for (int t = 0; t < 5; ++t) {
          const int a = t < 4 ? a0 + wave * 128 + (((t + (p >> 1) * 4) ^ (((r + wave) >> 1) & 3)) << 4) : a0 + 1024 + ((((wave & 3) * 2 + (p >> 1)) ^ ((r >> 1) & 3)) << 4);
#pragma unroll
          for (int ks = 0; ks < 2; ++ks) {
            const u32x2 lo = trd(lds, a + ks * 36864), hi = trd(lds, a + ks * 36864 + 9216);
            wdf[ks][t] = u32x4{lo[0], lo[1], hi[0], hi[1]};
          }
        }
#pragma unroll
        for (int t = 0; t < 5; ++t) asm volatile("" : "+v"(wdf[0][t]), "+v"(wdf[1][t]));
      }
      TSC(1);
      asm volatile("s_waitcnt vmcnt(0)\n\ts_barrier" ::: "memory");
      x8 = ld128(rs, xa); x1 = ld32(rs, xb);
      if (wave == 7) fy = ld128(rs, fo);
      slab_frags(lds, L_W, wave, lane, bu);
      STAMP(13);
#pragma unroll
      for (int s = 0; s < 9; ++s) asm volatile("" : "+v"(bu[s]));
    }
    TSC(2);
    {
      if (!FOK(fy)) {
        int sp = 0;
        _Pragma("nounroll") do { nap(2); fy = ld128(rs, fo); } while (!FOK(fy) && ++sp < SPIN_CAP);
      }
      TSC(3);
#define XOK() __all(mxu(mx4(x8), x1) != 0xffffffffu)
      if (!XOK()) {
        int sp = 0;
        _Pragma("nounroll") do { nap(1); x8 = ld128(rs, xa); x1 = ld32(rs, xb); } while (!XOK() && ++sp < SPIN_CAP);
      }
      STAMP(9); TSC(4);
      u32x4 h8; u32 h1;
      ln9(x8, pick2(x1, lane), cb1, sb, h8, h1);
      *(u32x4*)(lds + ia[0]) = h8;
      *(u16*)(lds + ia[1]) = (u16)h1;
      TSC(5);
      lds_barrier();
      u32x4 af[9];
#pragma unroll
      for (int s = 0; s < 9; ++s) { af[s] = *(const u32x4*)(lds + ra[s & 3] + s * 1024); SCHED(); }
      f32x4 ag = {0.f, 0.f, 0.f, 0.f}, au = {0.f, 0.f, 0.f, 0.f};
#pragma unroll
      for (int s = 0; s < 9; ++s) ag = mfma(bg[s], af[s], ag);
      SCHED();
#pragma unroll
      for (int s = 0; s < 9; ++s) au = mfma(bu[s], af[s], au);
      const float g = rbf(silu(rbf(half_sum1(ag, n < 8))));
#pragma unroll
      for (int s = 0; s < 9; ++s) { __builtin_amdgcn_sched_group_barrier(0x8, 1, 0); __builtin_amdgcn_sched_group_barrier(0x2, 3, 0); }
      SCHED();
      *(u16*)(lds + tw) = (u16)cvt2(g * rbf(half_sum1(au, n < 8)), 0.f);
    }
    lds_barrier();
    STAMP(10); TSC(6);
    {
      u32x4 af[2];
#pragma unroll
      for (int ks = 0; ks < 2; ++ks) af[ks] = *(const u32x4*)(lds + tr_ + ks * 64);
      f32x4 d[5];
#pragma unroll
      for (int t = 0; t < 4; ++t) d[t] = mfma(af[1], wdf[1][t], mfma(af[0], wdf[0][t], f32x4{0.f, 0.f, 0.f, 0.f}));
#pragma unroll
      for (int j = 0; j < 2; ++j)
        st128(rs, yo[2 * j], u32x4{sanf(d[0][j]), sanf(d[1][j]), sanf(d[2][j]), sanf(d[3][j])});
      SCHED();
      if (wave < 4) {
        d[4] = mfma(af[1], wdf[1][4], mfma(af[0], wdf[0][4], f32x4{0.f, 0.f, 0.f, 0.f}));
        __builtin_amdgcn_raw_buffer_store_b32(sanf(d[4][0]), rs, yo[1], 0, 16);
        __builtin_amdgcn_raw_buffer_store_b32(sanf(d[4][1]), rs, yo[1], 18 * NG * 128, 16);
      }
      TSC(7);
    }
  }
  {
  int t3_ = threadIdx.x; asm volatile("" : "+v"(t3_));
  const int tid = t3_, lane = tid & 63, n = lane & 15, grp = lane >> 4;
  const u32 t6_ = (u32)__builtin_amdgcn_s_memrealtime();
  if (idx < 12 && wave >= 6) {
    const int g0 = ((3 - (int)((uintptr_t)wu >> 7) - xcd * 9) & 7) + (wave - 6) * 8;
    UNTIL(t6_ + 15);
    if (g0 < 9) dma_keep(wu, (idx * 48 + min(lane, 47)) * 9216 + (xcd * 9 + g0) * 128, lds_base + L_B + 4096 + wave * 256);
  }
  if ((qs >= 0 || mw >= 6) && wave >= 2) {
    const int py = qs >= 0 ? qs : 58 + mw;
    _Pragma("nounroll") while ((int)((u32)__builtin_amdgcn_s_memrealtime() - t6_) < 20) __builtin_amdgcn_s_sleep(2);
#pragma unroll
    for (int i = 0; i < 3; ++i) {
      const int k_ = i * 384 + tid - 128, g_ = py * PY + k_;
      if (k_ < PY && g_ < 144 * NG * 8) st128(rs, W_Y + g_ * 16, u32x4{0xffffffffu, 0xffffffffu, 0xffffffffu, 0xffffffffu});
    }
    WAITV(0);
    if (lane == 0 && __hip_atomic_fetch_add((__attribute__((address_space(3))) u32*)((lds_t*)lds + L_B + 512), 1u, __ATOMIC_RELAXED, __HIP_MEMORY_SCOPE_WORKGROUP) == 5u)
      st64(rs, W_FY + py * 8, u32x2{e0, e1});
  }
  if (ml >= 0) {
    BAR();
    slab_dma(w_mod + ml * 64, NMOD * 2, lds_base + L_W, wave, lane);
    u32x4 wf[9], af[9];
    img_frags(lds, L_IMG, lane, af);
    asm volatile("s_waitcnt vmcnt(0)\n\ts_barrier" ::: "memory");
    slab_frags(lds, L_W, wave, lane, wf);
    const f32x4 s = half_sum(gemv9(wf, af));
    if (n < 8 && grp < 2) *(u32x2*)(lds + L_TILE + n * 128 + wave * 16 + grp * 8) = u32x2{cvt2(s[0], s[1]), cvt2(s[2], s[3])};
    lds_barrier();
    MPUB(ml);
  }

  if (idx >= 5 && idx < 14 && wave == 0) {
    const int b = xcd, sl = idx - 5, g = lane & 7;
    const rsrc_t rx = __builtin_amdgcn_make_buffer_rsrc((void*)x, 0, 8 * D * 2, 0x00020000);
    float xv[2], ga[2];
#pragma unroll
    for (int i = 0; i < 2; ++i) xv[i] = b2f(__builtin_amdgcn_raw_buffer_load_b16(rx, (b * D + sl * 64 + g * 8 + i * 4 + grp) * 2, 0, 0));
    u32 mm[2];
    _Pragma("nounroll") for (int sp = 0; sp < SPIN_CAP; ++sp) {
#pragma unroll
      for (int i = 0; i < 2; ++i) mm[i] = ld32(rs, W_M + b * 1152 + sl * 128 + g * 16 + i * 8 + (grp >> 1) * 4);
      if (__all(mxu(mm[0], mm[1]) != 0xffffffffu)) break;
      nap(4);
    }
    ga[0] = pick2(mm[0], grp); ga[1] = pick2(mm[1], grp);
    int xi = W_XP + (b * 9 + sl) * 2048 + lane * 16, xo = W_X1 + ((lane >> 3) * 8 + b) * 1152 + sl * 128 + g * 16;
    asm volatile("" : "+v"(xv[0]), "+v"(xv[1]), "+v"(ga[0]), "+v"(ga[1]), "+v"(xi), "+v"(xo));
    TSC(0);
    u32x4 xp[2];
    _Pragma("nounroll") for (int sp = 0; sp < SPIN_CAP; ++sp) {
      xp[0] = ld128(rs, xi); xp[1] = ld128(rs, xi + 1024);
      if (__all(mxu(mx4(xp[0]), mx4(xp[1])) != 0xffffffffu)) break;
      nap(1);
    }
    TSC(1);
    const u32x2 r0 = gather4(rbf(xv[0] + rbf(ga[0] * rbf(red8(asf4(xp[0])))))), r1 = gather4(rbf(xv[1] + rbf(ga[1] * rbf(red8(asf4(xp[1]))))));
    st128(rs, xo, u32x4{sanu(r0[0]), sanu(r0[1]), sanu(r1[0]), sanu(r1[1])});
    if (lane == 0) st128(rs, W_TS + (sl * 8 + b) * 128, u32x4{(u32)__builtin_amdgcn_s_memrealtime(), 0u, e0, e1});
    STAMP(8); TSC(2);
  }

  if (idx >= 5 && idx < 23 && wave == 1) {
    const int b = xcd, jh = idx - 5, col = jh * 32 + (jh < 16 ? (lane & 7) + grp * 8 : (lane & 7) * 4 + grp);
    const rsrc_t rw_ = __builtin_amdgcn_make_buffer_rsrc((void*)w_r, 0, D * 2, 0x00020000);
    const u32x4 wr8 = __builtin_amdgcn_raw_buffer_load_b128(rw_, lane * 16, 0, 0);
    const float wr1 = b2f(__builtin_amdgcn_raw_buffer_load_b16(rw_, 1024 + lane * 2, 0, 0));
    u32x4 mg[2]; u32 mh[2];
    _Pragma("nounroll") for (int sp = 0; sp < SPIN_CAP; ++sp) { MLD(b); if (MOK()) break; nap(4); }
    float sb[9], cb1[9];
    MUNP();
    const int xa = W_X1 + (xcd * 8 + b) * 1152 + lane * 16, xb = W_X1 + (xcd * 8 + b) * 1152 + 1024 + (lane >> 1) * 4;
    u32x4 x8; u32 x1;
    _Pragma("nounroll") for (int sp = 0; sp < SPIN_CAP; ++sp) { x8 = ld128(rs, xa); x1 = ld32(rs, xb); if (XOK()) break; nap(1); }
    const u32 tx_ = (u32)__builtin_amdgcn_s_memrealtime();
    const int go = W_M + (24 + b) * 1152 + (col >> 1) * 4, fo = W_FY + min(lane, NPY / 2 - 1) * 16;
    u32 gq = ld32(rs, go); u32x4 fy = ld128(rs, fo);
    const float v8 = pick2(x1, lane);
    u32x4 h8; u32 h1;
    ln9(x8, v8, cb1, sb, h8, h1);
    unsigned char* row = lds + L_C;
    *(u32x4*)(row + lane * 16) = x8; *(u32x4*)(row + 1152 + lane * 16) = h8;
    *(u16*)(row + 1024 + lane * 2) = (u16)(__float_as_uint(v8) >> 16); *(u16*)(row + 2176 + lane * 2) = (u16)h1;
    float r = lo16(h1) * wr1;
#pragma unroll
    for (int i = 0; i < 4; ++i) r = fmaf(hi16(h8[i]), hi16(wr8[i]), fmaf(lo16(h8[i]), lo16(wr8[i]), r));
    r = rbf(wsum(r));
    float rsel = rbf(1.f / (1.f + __expf(-r)));
    float x1v = b2f(*(const u16*)(row + col * 2)), h2 = b2f(*(const u16*)(row + 1152 + col * 2));
    _Pragma("nounroll") for (int sp = 0; sp < SPIN_CAP && !(__all(gq != 0xffffffffu) && FOK(fy)); ++sp) { nap(2); gq = ld32(rs, go); fy = ld128(rs, fo); }
    float gbv = pick2(gq, col);
    u32 tgo;
    {
      const u32x4 tq = ld128(rs, W_TS + lane * 128);
      int d_ = tag_ok(tq, e0, e1) ? (int)(tq[0] - tx_) : 0;
      d_ = max(d_, (int)dppu<0xB1>(d_)); d_ = max(d_, (int)dppu<0x4E>(d_)); d_ = max(d_, (int)dppu<0x141>(d_)); d_ = max(d_, (int)dppu<0x140>(d_));
      d_ = max(max(__builtin_amdgcn_readlane(d_, 0), __builtin_amdgcn_readlane(d_, 16)), max(__builtin_amdgcn_readlane(d_, 32), __builtin_amdgcn_readlane(d_, 48)));
      tgo = tx_ + min(d_ + TGO, TGO - 15);
    }
    int yi = W_Y + (b * 18 + jh) * NG * 128 + lane * 16, oo = (b * D + col) * 2;
    asm volatile("" : "+v"(rsel), "+v"(x1v), "+v"(h2), "+v"(gbv), "+v"(yi), "+v"(oo));
    TSC(5);
    UNTIL(tgo);
    float z;
    _Pragma("nounroll") for (int sp = 0; sp < SPIN_CAP; ++sp) {
      u32x4 y[9];
#pragma unroll
      for (int i = 0; i < 9; ++i) y[i] = ld128(rs, yi + i * 1024);
      z = red8((((asf4(y[0]) + asf4(y[1])) + (asf4(y[2]) + asf4(y[3]))) + ((asf4(y[4]) + asf4(y[5])) + (asf4(y[6]) + asf4(y[7])))) + asf4(y[8]));
      if (__all(z == z)) break;
      u32 m = mx4(y[8]);
#pragma unroll
      for (int i = 0; i < 8; ++i) m = mxu(m, mx4(y[i]));
      if (__all(m != 0xffffffffu)) break;
      nap(1);
    }
    STAMPW(11); TSC(6);
    const float y2 = rbf(rbf(z) * rsel);
    const float res = x1v + rbf(gbv * rbf(h2 + y2));
    if (!(lane & 8)) __builtin_amdgcn_raw_buffer_store_b16((u16)(cvt2(res, 0.f) & 0xffffu), __builtin_amdgcn_make_buffer_rsrc(out, 0, 8 * D * 2, 0x00020000), oo, 0, 0);
    STAMPW(12); TSC(7);
  }
  {
    UNTIL(tstart + 3200);
    const u32x4 g_ = ld128(rs, W_FY + 896 + (lane & 7) * 16);
    int d_ = (int)(g_[0] - tstart);
    d_ = min(d_, (int)dppu<0xB1>(d_)); d_ = min(d_, (int)dppu<0x4E>(d_)); d_ = min(d_, (int)dppu<0x141>(d_));
    UNTIL(tstart + __builtin_amdgcn_readfirstlane(d_) + 4000);
  }
  TSOUT();
  }
  }
}

extern "C" void kernel_launch(void* const* d_in, const int* in_sizes, int n_in, void* d_out, int out_size, void* d_ws, size_t ws_size,
                              hipStream_t stream) {
  fused_kernel<<<NWG, NT, 0, stream>>>((const u16*)d_in[0], (const u16*)d_in[1], (const u16*)d_in[2], (const u16*)d_in[3], (const u16*)d_in[4],
                                       (const u16*)d_in[6], (const u16*)d_in[7], (const u16*)d_in[8], (const u16*)d_in[9], (const u16*)d_in[10],
                                       (const u16*)d_in[11], (unsigned char*)d_ws, (u16*)d_out);
}
```

```cpp
#include <hip/hip_runtime.h>
#include <hip/hip_bf16.h>
#include <cstdint>

typedef unsigned u32;
typedef unsigned short u16;
typedef u32 u32x4 __attribute__((ext_vector_type(4)));
typedef u32 u32x2 __attribute__((ext_vector_type(2)));
typedef float f32x4 __attribute__((ext_vector_type(4)));
typedef float f32x2 __attribute__((ext_vector_type(2)));
typedef short s16x4 __attribute__((ext_vector_type(4)));
typedef __bf16 bf16x8 __attribute__((ext_vector_type(8)));
typedef __bf16 bf16x16 __attribute__((ext_vector_type(16)));
typedef u32 u32x8 __attribute__((ext_vector_type(8)));
typedef __bf16 bf16x2 __attribute__((ext_vector_type(2)));
typedef __attribute__((address_space(3))) unsigned char lds_t;

constexpr int D = 576, SK = 8192, WIN = 2048, FF = 4608, NQ = 4096, NMOD = 6 * D;
constexpr int NWG = 256, NT = 512, NG = 72;
constexpr int SPIN_CAP = 1 << 15;

constexpr int W_O = 0;
constexpr int W_XP = W_O + 262144;
constexpr int W_X1 = W_XP + 147456;
constexpr int W_M = W_X1 + 82944;
constexpr int W_MS = W_M + 36864;
constexpr int W_Y = W_MS;
constexpr int W_FL = W_Y + 144 * NG * 128;
constexpr int W_FY = W_FL + 1024;
constexpr int W_H = W_FY + 1024;
constexpr int W_Q = W_H + 36864;
constexpr int W_ST = W_Q + 131072;
constexpr int W_TS = W_ST + 32768;
constexpr int W_END = W_TS + 72 * 128;
constexpr int PZ = (W_MS / 16 + NG - 1) / NG;
constexpr int NPY = 104;
static_assert(PZ <= NT && NPY * 13 * 1024 >= 144 * NG * 128, "poison split");

constexpr int L_W = 0;
constexpr int L_IMG = 73728;
constexpr int L_TILE = L_IMG + 9216;
constexpr int L_OI = 65536;
constexpr int L_B = 147456;
constexpr int L_C = 155648;
constexpr int LDS_BYTES = 163840;

#define STAMP(i) do {} while (0)
#define STAMPW(i) do {} while (0)

#define SCHED() __builtin_amdgcn_sched_barrier(0)
#define UNTIL(t) do { const u32 u_ = (t); _Pragma("nounroll") while ((int)((u32)__builtin_amdgcn_s_memrealtime() - u_) < 0) __builtin_amdgcn_s_sleep(1); asm volatile("" ::: "memory"); } while (0)
#define BAR() asm volatile("s_barrier" ::: "memory")
#define TS(i) do {} while (0)
#define TSOUT() do {} while (0)
#define TSA(i) do {} while (0)
#define TSB(i) do {} while (0)
#define TSC(i) do {} while (0)
#define WAITV_(n) do { asm volatile("s_waitcnt vmcnt(" #n ")" ::: "memory"); SCHED(); } while (0)
#define WAITV(n) WAITV_(n)
#define DK 5
#define DV DK
#define PRM(i, v) (v)
#define TXV 187
#define PRV 111
#define TGV 185
#define LFV 65
#define PR PRM(0, PRV)
#define TX PRM(1, TXV)
#define LF PRM(2, LFV)
#define LO PRM(3, 30)
#define E60 PRM(9, 10)
#define E62 PRM(10, 20)
#define H1 PRM(11, 2)
#define H2 PRM(12, 5)
#define H3 PRM(13, 5)
#define HD2 PRM(14, 10)
#define HD3 PRM(15, 70)
#define HS2 PRM(16, 6)
#define OD PRM(17, 70)
#define TGO PRM(18, TGV)
#define TSL PRM(19, 220)
#define FSL PRM(4, 390)
#define RD PRM(5, 30)
#define OUTAUX 0
#define NB 4
#define UA 5
#define PAT 13, 15, 11, 16, 9, 12, 14, 8, 10, 18, 17
constexpr int AB[11] = {PAT};

__device__ __forceinline__ float lo16(u32 u) { return __uint_as_float(u << 16); }
__device__ __forceinline__ float hi16(u32 u) { return __uint_as_float(u & 0xffff0000u); }
__device__ __forceinline__ float b2f(u16 u) { return __uint_as_float((u32)u << 16); }
__device__ __forceinline__ u32 cvt2(float a, float b) { return __builtin_bit_cast(u32, __builtin_convertvector(f32x2{a, b}, bf16x2)); }
__device__ __forceinline__ float rbf(float v) { return __uint_as_float(cvt2(0.f, v)); }
__device__ __forceinline__ u32 sanf(float f) { u32 v = __float_as_uint(f); return v < 0xfffffffeu ? v : 0xfffffffeu; }
__device__ __forceinline__ u32 sanu(u32 v) { return v < 0xfffffffeu ? v : 0xfffffffeu; }
__device__ __forceinline__ f32x4 asf4(u32x4 v) { return __builtin_bit_cast(f32x4, v); }
__device__ __forceinline__ f32x2 up2(u32 u) { return f32x2{lo16(u), hi16(u)}; }
__device__ __forceinline__ f32x2 rbf2(f32x2 v) { return f32x2{rbf(v.x), rbf(v.y)}; }
__device__ __forceinline__ void lds_barrier() { asm volatile("s_waitcnt lgkmcnt(0)\n\ts_barrier" ::: "memory"); }
__device__ __forceinline__ f32x4 mfma(u32x4 a, u32x4 b, f32x4 c) {
  return __builtin_amdgcn_mfma_f32_16x16x32_bf16(__builtin_bit_cast(bf16x8, a), __builtin_bit_cast(bf16x8, b), c, 0, 0, 0);
}
__device__ __forceinline__ f32x4 smf(u32x4 a, u32x8 b, f32x4 c, int ix) {
  return __builtin_amdgcn_smfmac_f32_16x16x64_bf16(__builtin_bit_cast(bf16x8, a), __builtin_bit_cast(bf16x16, b), c, ix, 0, 0);
}
__device__ __forceinline__ f32x2 smf_sum(f32x4 d) { return f32x2{d[0], d[1]} + f32x2{d[2], d[3]}; }
__device__ __forceinline__ u32 plo(u32 a, u32 b) { return __builtin_amdgcn_perm(b, a, 0x05040100u); }
__device__ __forceinline__ u32 phi(u32 a, u32 b) { return __builtin_amdgcn_perm(b, a, 0x07060302u); }

extern "C" __device__ __attribute__((const)) unsigned long long llvm_dispatch_id() __asm("llvm.amdgcn.dispatch.id");
typedef __amdgpu_buffer_rsrc_t rsrc_t;
__device__ __forceinline__ u32x4 ld128(rsrc_t rs, int off) { return __builtin_amdgcn_raw_buffer_load_b128(rs, off, 0, 16); }
__device__ __forceinline__ u32 ld32(rsrc_t rs, int off) { return __builtin_amdgcn_raw_buffer_load_b32(rs, off, 0, 16); }
__device__ __forceinline__ void st128(rsrc_t rs, int off, u32x4 v) { __builtin_amdgcn_raw_buffer_store_b128(v, rs, off, 0, 16); }
__device__ __forceinline__ void st64(rsrc_t rs, int off, u32x2 v) { __builtin_amdgcn_raw_buffer_store_b64(v, rs, off, 0, 16); }
__device__ __forceinline__ void dma(const void* gbase, u32 voff, unsigned lds_dst) {
  unsigned keep;
  asm volatile("s_mov_b32 %0, m0\n\ts_mov_b32 m0, %3\n\ts_nop 2\n\tglobal_load_lds_dwordx4 %1, %2 nt\n\ts_mov_b32 m0, %0"
               : "=&s"(keep) : "v"(voff), "s"(gbase), "s"(lds_dst) : "memory");
}
__device__ __forceinline__ void dma_keep(const void* gbase, u32 voff, unsigned lds_dst) {
  unsigned keep;
  asm volatile("s_mov_b32 %0, m0\n\ts_mov_b32 m0, %3\n\ts_nop 2\n\tglobal_load_lds_dword %1, %2\n\ts_mov_b32 m0, %0"
               : "=&s"(keep) : "v"(voff), "s"(gbase), "s"(lds_dst) : "memory");
}
__device__ __forceinline__ void dma_keep64(const void* addr, unsigned lds_dst) {
  unsigned keep;
  asm volatile("s_mov_b32 %0, m0\n\ts_mov_b32 m0, %2\n\ts_nop 2\n\tglobal_load_lds_dword %1, off\n\ts_mov_b32 m0, %0"
               : "=&s"(keep) : "v"(addr), "s"(lds_dst) : "memory");
}
__device__ __forceinline__ void dma_sc(const void* gbase, u32 voff, unsigned lds_dst) {
  unsigned keep;
  asm volatile("s_mov_b32 %0, m0\n\ts_mov_b32 m0, %3\n\ts_nop 2\n\tglobal_load_lds_dwordx4 %1, %2 sc1\n\ts_mov_b32 m0, %0"
               : "=&s"(keep) : "v"(voff), "s"(gbase), "s"(lds_dst) : "memory");
}
__device__ __forceinline__ void ald128sc(u32x4& d, const void* gbase, u32 voff) { unsigned long long t; asm volatile("s_mov_b64 %1, %3\n\tglobal_load_dwordx4 %0, %2, %1 sc1" : "=v"(d), "=&s"(t) : "v"(voff), "s"(gbase) : "memory"); }
__device__ __forceinline__ u32x2 trd(const unsigned char* lds, int off) {
  return __builtin_bit_cast(u32x2, __builtin_amdgcn_ds_read_tr16_b64_v4i16((__attribute__((address_space(3))) s16x4*)((lds_t*)lds + off)));
}
#define nap(n) do { __builtin_amdgcn_s_sleep(n); asm volatile("" ::: "memory"); } while (0)
__device__ __forceinline__ float frcp(float v) { return __builtin_amdgcn_rcpf(v); }
__device__ __forceinline__ float fdiv(float a, float d, float r) { const float q = a * r; return fmaf(fmaf(-q, d, a), r, q); }
__device__ __forceinline__ float silu(float v) { const float d = 1.f + __expf(-v); const float r = frcp(d); return d < 3.0e38f ? fdiv(v, d, r) : v * r; }
template <int CTRL> __device__ __forceinline__ float dppf(float x) {
  return __builtin_bit_cast(float, __builtin_amdgcn_mov_dpp(__builtin_bit_cast(int, x), CTRL, 0xf, 0xf, true));
}
template <int CTRL> __device__ __forceinline__ u32 dppu(u32 x) { return (u32)__builtin_amdgcn_mov_dpp((int)x, CTRL, 0xf, 0xf, true); }
__device__ __forceinline__ u32 mxu(u32 a, u32 b) { return a > b ? a : b; }
__device__ __forceinline__ u32 mx4(u32x4 v) { return mxu(mxu(v[0], v[1]), mxu(v[2], v[3])); }
__device__ __forceinline__ float rl(float v, int l) { return __builtin_bit_cast(float, __builtin_amdgcn_readlane(__builtin_bit_cast(int, v), l)); }
__device__ __forceinline__ float wsum(float v) {
  v += dppf<0xB1>(v); v += dppf<0x4E>(v); v += dppf<0x141>(v); v += dppf<0x140>(v);
  return (rl(v, 0) + rl(v, 16)) + (rl(v, 32) + rl(v, 48));
}
__device__ __forceinline__ float up32(float v) { const u32 u = __float_as_uint(v); return __uint_as_float(__builtin_amdgcn_permlane32_swap(u, u, false, false)[1]); }
__device__ __forceinline__ float xsum32(float v) { const u32 u = __float_as_uint(v); const auto r = __builtin_amdgcn_permlane32_swap(u, u, false, false); return __uint_as_float(r[0]) + __uint_as_float(r[1]); }
__device__ __forceinline__ float xsum16(float v) { const u32 u = __float_as_uint(v); const auto r = __builtin_amdgcn_permlane16_swap(u, u, false, false); return __uint_as_float(r[0]) + __uint_as_float(r[1]); }
__device__ __forceinline__ float xmax32(float v) { const u32 u = __float_as_uint(v); const auto r = __builtin_amdgcn_permlane32_swap(u, u, false, false); return fmaxf(__uint_as_float(r[0]), __uint_as_float(r[1])); }
__device__ __forceinline__ float xmax16(float v) { const u32 u = __float_as_uint(v); const auto r = __builtin_amdgcn_permlane16_swap(u, u, false, false); return fmaxf(__uint_as_float(r[0]), __uint_as_float(r[1])); }
__device__ __forceinline__ float red8(f32x4 v) {
  const auto a = __builtin_amdgcn_permlane32_swap(__float_as_uint(v[0]), __float_as_uint(v[2]), false, false);
  const auto b = __builtin_amdgcn_permlane32_swap(__float_as_uint(v[1]), __float_as_uint(v[3]), false, false);
  const float w0 = __uint_as_float(a[0]) + __uint_as_float(a[1]), w1 = __uint_as_float(b[0]) + __uint_as_float(b[1]);
  const auto c = __builtin_amdgcn_permlane16_swap(__float_as_uint(w0), __float_as_uint(w1), false, false);
  const float z = __uint_as_float(c[0]) + __uint_as_float(c[1]);
  return z + dppf<0x128>(z);
}
__device__ __forceinline__ u32x2 gather4(float z) {
  const u32 u = __float_as_uint(z);
  const auto a = __builtin_amdgcn_permlane16_swap(u, u, false, false);
  const u32 pk = phi(a[0], a[1]);
  const auto b = __builtin_amdgcn_permlane32_swap(pk, pk, false, false);
  return u32x2{b[0], b[1]};
}
__device__ __forceinline__ f32x4 half_sum(f32x4 d) {
  f32x4 r;
#pragma unroll
  for (int j = 0; j < 4; ++j) r[j] = d[j] + dppf<0x108>(up32(d[j]));
  return r;
}
__device__ __forceinline__ float half_sum1(f32x4 d, bool lo) {
  const auto a = __builtin_amdgcn_permlane32_swap(__float_as_uint(d[0]), __float_as_uint(d[2]), false, false);
  const auto b = __builtin_amdgcn_permlane32_swap(__float_as_uint(d[1]), __float_as_uint(d[3]), false, false);
  return __uint_as_float(lo ? a[0] : b[1]) + dppf<0x128>(__uint_as_float(lo ? b[0] : a[1]));
}
__device__ __forceinline__ void unpack8(u32x4 u, float* f) {
#pragma unroll
  for (int j = 0; j < 4; ++j) { f[2 * j] = lo16(u[j]); f[2 * j + 1] = hi16(u[j]); }
}
__device__ __forceinline__ void ln_row(const float (&v)[16], bool has2, float& mean, float& den) {
  float s = 0.f;
#pragma unroll
  for (int i = 0; i < 8; ++i) s += v[i];
  if (has2) {
#pragma unroll
    for (int i = 8; i < 16; ++i) s += v[i];
  }
  s = wsum(s);
  const float mu = s * (1.f / D);
  mean = rbf(mu);
  float q = 0.f;
#pragma unroll
  for (int i = 0; i < 8; ++i) { const float t = v[i] - mu; q = fmaf(t, t, q); }
  if (has2) {
#pragma unroll
    for (int i = 8; i < 16; ++i) { const float t = v[i] - mu; q = fmaf(t, t, q); }
  }
  q = wsum(q);
  const float var = rbf(q * (1.f / D));
  den = rbf(sqrtf(rbf(var + 1e-5f)));
}
__device__ __forceinline__ float pick2(u32 t, int e) { return (e & 1) ? hi16(t) : lo16(t); }
__device__ __forceinline__ void ln9(u32x4 x8, float v8, const float (&cb1)[9], const float (&sb)[9], u32x4& h8, u32& h1) {
  f32x2 v[4];
#pragma unroll
  for (int i = 0; i < 4; ++i) v[i] = up2(x8[i]);
  const f32x2 s2 = (v[0] + v[1]) + (v[2] + v[3]);
  const float mu = wsum((s2.x + s2.y) + v8) * (1.f / D);
  const f32x2 mu2 = {mu, mu};
  const float t8 = v8 - mu;
  f32x2 q = {t8 * t8, 0.f};
#pragma unroll
  for (int i = 0; i < 4; ++i) { const f32x2 t = v[i] - mu2; q = __builtin_elementwise_fma(t, t, q); }
  const float mean = rbf(mu);
  f32x2 d[4];
#pragma unroll
  for (int i = 0; i < 4; ++i) d[i] = rbf2(v[i] - f32x2{mean, mean});
  const float d8 = rbf(v8 - mean);
  const float var = rbf(wsum(q.x + q.y) * (1.f / D));
  const float rden = frcp(rbf(__builtin_amdgcn_sqrtf(rbf(var + 1e-5f))));
#pragma unroll
  for (int i = 0; i < 4; ++i) {
    const f32x2 t = rbf2(rbf2(d[i] * f32x2{rden, rden}) * f32x2{cb1[2 * i], cb1[2 * i + 1]}) + f32x2{sb[2 * i], sb[2 * i + 1]};
    h8[i] = cvt2(t.x, t.y);
  }
  h1 = cvt2(rbf(rbf(d8 * rden) * cb1[8]) + sb[8], 0.f);
}
__device__ __forceinline__ void ln9f(u32x4 x8, float v8, const float (&cb1)[9], const float (&sb)[9], u32x4& h8, u32& h1) {
  const float s0 = lo16(__builtin_amdgcn_readfirstlane(x8[0]));
  f32x2 d[4];
#pragma unroll
  for (int i = 0; i < 4; ++i) d[i] = up2(x8[i]) - f32x2{s0, s0};
  const float d8 = v8 - s0;
  const f32x2 s2 = (d[0] + d[1]) + (d[2] + d[3]);
  const f32x2 q2 = __builtin_elementwise_fma(d[0], d[0], d[1] * d[1]) + __builtin_elementwise_fma(d[2], d[2], d[3] * d[3]);
  const auto r = __builtin_amdgcn_permlane32_swap(__float_as_uint((s2.x + d8) + s2.y), __float_as_uint(fmaf(d8, d8, q2.x) + q2.y), false, false);
  float z = __uint_as_float(r[0]) + __uint_as_float(r[1]);
  z += dppf<0xB1>(z); z += dppf<0x4E>(z); z += dppf<0x141>(z); z += dppf<0x140>(z);
  f32x2 t[4];
#pragma unroll
  for (int i = 0; i < 4; ++i) t[i] = d[i] * f32x2{cb1[2 * i], cb1[2 * i + 1]};
  const float md = (rl(z, 0) + rl(z, 16)) * (1.f / D);
  const float var = rbf(fmaf(-md, md, (rl(z, 32) + rl(z, 48)) * (1.f / D)));
  const float mr = s0 - rbf(s0 + md);
  const float rden = frcp(rbf(__builtin_amdgcn_sqrtf(rbf(var + 1e-5f))));
#pragma unroll
  for (int i = 0; i < 4; ++i) {
    const f32x2 u = __builtin_elementwise_fma(__builtin_elementwise_fma(f32x2{mr, mr}, f32x2{cb1[2 * i], cb1[2 * i + 1]}, t[i]), f32x2{rden, rden}, f32x2{sb[2 * i], sb[2 * i + 1]});
    h8[i] = cvt2(u.x, u.y);
  }
  h1 = cvt2(fmaf((d8 + mr) * cb1[8], rden, sb[8]), 0.f);
}
template <int J0 = 0, int J1 = 9>
__device__ __forceinline__ void slab_dma(const u16* base, int stride_b, unsigned lds_dst, int wave, int lane) {
  const u32 v0 = (lane >> 3) * stride_b + (((lane & 7) ^ ((wave & 1) * 4 + (lane >> 4))) << 4), v1 = v0 ^ 32u;
  const u32 v4 = wave >= 4 ? v1 : v0;
  const char* gb = (const char*)base + (size_t)wave * 8 * stride_b;
#pragma unroll
  for (int j = J0; j < J1; ++j) dma(gb + (size_t)j * 64 * stride_b, j < 4 ? v0 : j == 4 ? v4 : v1, lds_dst + (j * 8 + wave) * 1024);
}
__device__ __forceinline__ void slab_frags(const unsigned char* lds, int base, int wave, int lane, u32x4 (&wf)[9]) {
  const int g = lane >> 4, q = (lane & 15) >> 2, p = lane & 3, hf = p >> 1;
  const int a0 = base + (hf * 288 + 8 * g + q) * 128 + ((wave ^ ((g & 1) * 4 + (q >> 1)) ^ (hf * 2)) << 4) + (p & 1) * 8, a1 = (a0 ^ 32) + 512;
#pragma unroll
  for (int s = 0; s < 9; ++s) {
    const u32x2 lo = trd(lds, a0 + s * 4096), hi = trd(lds, a1 + s * 4096);
    wf[s] = u32x4{lo[0], lo[1], hi[0], hi[1]};
  }
}
__device__ __forceinline__ void img_frags(const unsigned char* lds, int img, int lane, u32x4 (&af)[9]) {
#pragma unroll
  for (int s = 0; s < 9; ++s) af[s] = *(const u32x4*)(lds + img + (s * 64 + lane) * 16);
}
__device__ __forceinline__ f32x4 gemv9(const u32x4 (&wf)[9], const u32x4 (&af)[9]) {
  f32x4 acc[3];
  SCHED();
#pragma unroll
  for (int s = 0; s < 9; ++s) acc[s % 3] = mfma(wf[s], af[s], s < 3 ? f32x4{0.f, 0.f, 0.f, 0.f} : acc[s % 3]);
  return (acc[0] + acc[1]) + acc[2];
}
__device__ __forceinline__ int img_off(int b, int k8) {
  const int hf = k8 >= 36 ? 1 : 0, kk = k8 - hf * 36;
  return ((kk >> 2) * 64 + (kk & 3) * 16 + hf * 8 + b) * 16;
}
__device__ __forceinline__ u32 tag_x(u32x4 v, u32 e0, u32 e1) { return (v[2] ^ e0) | (v[3] ^ e1); }
__device__ __forceinline__ bool tag_ok(u32x4 v, u32 e0, u32 e1) { return tag_x(v, e0, e1) == 0u; }

#define CODEB 27904
constexpr int CODE_BYTES = CODEB;

__global__ void __launch_bounds__(NT, 2) fused_kernel(const u16* __restrict__ x, const u16* __restrict__ kc, const u16* __restrict__ vc,
                                                      const u16* __restrict__ wq, const u16* __restrict__ wo, const u16* __restrict__ w_r,
                                                      const u16* __restrict__ wg, const u16* __restrict__ wu, const u16* __restrict__ wd,
                                                      const u16* __restrict__ c, const u16* __restrict__ w_mod, unsigned char* ws, u16* out) {
  __shared__ __attribute__((aligned(16))) unsigned char lds[LDS_BYTES];
  int w = blockIdx.x;
  const int tid = threadIdx.x, lane = tid & 63, wave = __builtin_amdgcn_readfirstlane(tid >> 6);
  const int n = lane & 15, grp = lane >> 4;
  const size_t pairbase = ((size_t)(w >> 1) * SK + (SK - WIN)) * 256 + ((w & 1) << AB[8]);
  const int wpart = ((wave & 1) << AB[5]) + (((wave >> 1) & 1) << AB[6]) + ((wave >> 2) << AB[7]);
  int ko[4];
  ko[0] = wpart + (((n >> 2) & 1) << AB[0]) + ((n >> 3) << AB[1]) + grp * 64 + (n & 3) * 16;
  ko[1] = ko[0] + (1 << AB[2]); ko[2] = ko[0] + (1 << AB[3]); ko[3] = ko[0] + (1 << AB[2]) + (1 << AB[3]);
  u32x4 kb[32], vb[32];
#define KLD(u) do { unsigned long long t_; asm volatile("s_mov_b64 %1, %3\n\tglobal_load_dwordx4 %0, %2, %1 nt" : "=v"(kb[u]), "=&s"(t_) : "v"(ko[(u) & 3]), "s"(kp + ((((u) >> 2) & 1) << AB[4]) + ((((u) >> 3) & 1) << AB[9]) + (((u) >> 4) << AB[10])) : "memory"); } while (0)
  unsigned long long tst;
  const char* kp = (const char*)kc + pairbase;
  asm volatile("s_memrealtime %0\n\tglobal_load_dwordx4 %1, %5, %9 nt\n\tglobal_load_dwordx4 %2, %6, %9 nt\n\t"
               "global_load_dwordx4 %3, %7, %9 nt\n\tglobal_load_dwordx4 %4, %8, %9 nt"
               : "=&s"(tst), "=&v"(kb[0]), "=&v"(kb[1]), "=&v"(kb[2]), "=&v"(kb[3]) : "v"(ko[0]), "v"(ko[1]), "v"(ko[2]), "v"(ko[3]), "s"(kp) : "memory");
  asm volatile("" : "+s"(vc), "+s"(w_r), "+s"(wu), "+s"(wd), "+s"(out), "+s"(x), "+s"(w_mod), "+s"(wg), "+s"(wq), "+s"(wo), "+s"(c));
  asm volatile("s_waitcnt lgkmcnt(0)" : "+s"(w), "+s"(ws), "+s"(tst));
  const u32 tstart = (u32)tst;
  const int xcd = w & 7, idx = w >> 3;
  const rsrc_t rs = __builtin_amdgcn_make_buffer_rsrc(ws, 0, W_END, 0x00020000);
  const unsigned lds_base = (unsigned)(uintptr_t)(lds_t*)lds;
  const unsigned long long epoch = (llvm_dispatch_id() + 1ull) * 0x9E3779B97F4A7C15ull;
  const u32 e0 = (u32)epoch, e1 = (u32)(epoch >> 32);
  unsigned long long pc0; asm volatile("s_getpc_b64 %0" : "=s"(pc0));

  const int me = idx < 6 ? xcd * 6 + idx : -1;
  const int ml = idx == 0 && xcd < 6 ? 48 + xcd : -1;
  const int qs = idx >= 6 && idx < 14 ? xcd * 8 + idx - 6 : -1;
  const int oj = idx >= 14 && idx < 23 ? idx - 14 : -1, op = xcd;
  const int gs = idx >= 23 ? xcd * 9 + idx - 23 : -1;

  const int s_att = w & 1, kvh = (w >> 1) & 15, b_att = w >> 5;
  {
    const char* vp = (const char*)vc + pairbase;
    int vo[4];
#define VLD(u) do { unsigned long long t_; asm volatile("s_mov_b64 %1, %3\n\tglobal_load_dwordx4 %0, %2, %1 nt" : "=v"(vb[u]), "=&s"(t_) : "v"(vo[(u) & 3]), "s"(vp + ((((u) >> 2) & 1) << AB[4]) + ((((u) >> 3) & 1) << AB[9]) + (((u) >> 4) << AB[10])) : "memory"); } while (0)
#define TIE8(a, o) asm volatile("" : "+v"(a[o]), "+v"(a[(o) + 1]), "+v"(a[(o) + 2]), "+v"(a[(o) + 3]), "+v"(a[(o) + 4]), "+v"(a[(o) + 5]), "+v"(a[(o) + 6]), "+v"(a[(o) + 7]))
    const int pr_ = PR;
    const u32 tp0 = tstart + TX - (gs >= 0 ? LF : oj >= 0 ? LO : 0);
#define PACE(i) do { _Pragma("nounroll") while ((int)((u32)__builtin_amdgcn_s_memrealtime() - tp0) < (i) * pr_ / 4) __builtin_amdgcn_s_sleep(2); SCHED(); } while (0)
    static_assert(DK <= 7 && DV <= 7, "the hand-placed waits assume it");
#define POISON() do { if (gs >= 0) { const int g_ = gs * PZ + tid; \
      if (tid < PZ && g_ < W_MS / 16) st128(rs, g_ * 16, u32x4{0xffffffffu, 0xffffffffu, 0xffffffffu, 0xffffffffu}); } SCHED(); } while (0)
#define PFLAG() do { if (gs >= 0 && tid == 448) st64(rs, W_FL + gs * 8, u32x2{e0, e1}); SCHED(); } while (0)
#define FOK(a) __all(((a[0] ^ e0) | (a[1] ^ e1) | tag_x(a, e0, e1)) == 0u)
#define FREQ() do { if (wave == 1) dma_sc(ws + W_FL, min(lane, NG / 2 - 1) * 16, lds_base + L_B + 2048); } while (0)
#define FCHK() do { if (wave == 1 && !FOK((*(const u32x4*)(lds + L_B + 2048 + lane * 16)))) { \
      _Pragma("nounroll") for (int sp = 0; sp < SPIN_CAP; ++sp) { nap(4); const u32x4 a_ = ld128(rs, W_FL + min(lane, NG / 2 - 1) * 16); if (FOK(a_)) break; } } SCHED(); } while (0)
#define SILU_IMAGE(c0, c1) do { float f[8]; unpack8(c0, f); u32x4 o; \
        _Pragma("unroll") for (int j = 0; j < 4; ++j) o[j] = cvt2(silu(f[2 * j]), silu(f[2 * j + 1])); \
        const int b_ = tid / 72, ch_ = tid - b_ * 72; \
        *(u32x4*)(lds + L_IMG + img_off(b_, ch_)) = o; \
        if (wave == 0) { unpack8(c1, f); \
          _Pragma("unroll") for (int j = 0; j < 4; ++j) o[j] = cvt2(silu(f[2 * j]), silu(f[2 * j + 1])); \
          *(u32x4*)(lds + L_IMG + img_off(7, 8 + lane)) = o; } } while (0)
    {
      constexpr int L_LAND = 98304;
      const unsigned land = lds_base + L_LAND + wave * 6144;
      const unsigned char* lp = lds + L_LAND + wave * 6144 + lane * 16;
      if (me >= 0) {
        dma_sc(c, tid * 16, land);
        if (wave == 0) dma_sc(c, (512 + lane) * 16, land + 1024);
        if (me >= 9 && me < 18) { dma(x, wave * 1152 + lane * 16, land + 2048); dma(x, wave * 1152 + 1024 + (lane & 7) * 16, land + 3072); }
      }
      {
        const int cnt = 3 - (idx < 6) - 2 * (oj >= 0);
        const int r0 = 24 * idx - 8 * min(idx, 6) - 16 * (min(max(idx, 14), 23) - 14) + wave * cnt;
        const int r_ = (lane * 37) >> 9, c_ = lane - r_ * 14, hi = (7 - c_) >> 31;
        const u32 off = (r0 + r_) * (NQ * 2 - (hi & (NQ * 2 - NMOD * 2))) + xcd * 1024 - (hi & (xcd * 256 + 1024)) + c_ * 128;
        const unsigned long long on = (1ull << (cnt * 14)) - 1ull, mq = 0xff03fc0ffull;
        unsigned keep;
        asm volatile("s_mov_b32 %0, m0\n\ts_mov_b32 m0, %6\n\ts_mov_b64 exec, %4\n\ts_nop 2\n\tglobal_load_lds_dword %1, %2\n\ts_mov_b64 exec, %5\n\tglobal_load_lds_dword %1, %3\n\t"
                     "s_mov_b64 exec, -1\n\ts_mov_b32 m0, %0"
                     : "=&s"(keep) : "v"(off), "s"(wq), "s"(w_mod), "s"(on & mq), "s"(on & ~mq), "s"(lds_base + L_B + 4096 + wave * 256) : "memory");
      }
      POISON();
      if (oj >= 0 && oj < 8 && wave == 4)
        dma_keep((const void*)pc0, min((oj & 3) * 8192 + lane * 128, CODE_BYTES - 128), land);
      const u32 v0 = (lane >> 3) * 1152 + (((lane & 7) ^ (lane >> 4)) << 4);
      const char* gb = (const char*)(wo + (size_t)(op * 512 + wave * 64) * D + oj * 64);
      const bool fs = qs >= 0 || me >= 0;
      const u16* sbase = qs >= 0 ? wq + qs * 64 : w_mod + me * 64;
      const int sstr = qs >= 0 ? NQ * 2 : NMOD * 2;
      const unsigned sdst = lds_base + L_W;
      if (oj >= 0) {
#pragma unroll
        for (int j = NB; j < 8; ++j) dma(gb + j * 8 * 1152, v0 ^ ((j & 1) * 64), lds_base + L_W + (wave * 8 + j) * 1024);
      }
#define WK(j) do { if (!((j) & 1)) PACE((j) + 4); KLD((j) + 4); WAITV(DK); } while (0)
      static_assert(DK <= 5, "six younger requests cover the poison");
      TSA(0);
      float* st = (float*)(lds + L_TILE + 1024);
      if (me >= 0) {
        WAITV(2);
        const u32x4 c0 = *(const u32x4*)lp, c1 = *(const u32x4*)(lp + 1024); SILU_IMAGE(c0, c1);
        if (me >= 9 && me < 18) {
          float v[16]; unpack8(*(const u32x4*)(lp + 2048), v); unpack8(*(const u32x4*)(lp + 3072), v + 8);
          float mean, den; ln_row(v, lane < 8, mean, den);
          if (lane == 0) { st[wave] = mean; st[8 + wave] = den; }
        }
      }
      TSA(1);
      if (fs) { lds_barrier(); UNTIL(tstart + TSL); slab_dma(sbase, sstr, sdst, wave, lane); BAR(); }
      WK(0);
      KLD(5);
      if (me >= 0) {
        u32x4 wf[9], af[9];
        img_frags(lds, L_IMG, lane, af);
        WAITV(2); BAR();
        TSA(2);
        slab_frags(lds, L_W, wave, lane, wf);
        f32x4 r = half_sum(gemv9(wf, af));
        if (me >= 18) { if (n < 8 && grp < 2) *(u32x2*)(lds + L_TILE + n * 128 + wave * 16 + grp * 8) = u32x2{cvt2(r[0], r[1]), cvt2(r[2], r[3])}; }
        else {
          const int k8 = (me >= 9 ? me - 9 : me) * 8 + wave;
          if (me >= 9) {
            const u32x2 xc = *(const u32x2*)(lds + L_LAND + (n & 7) * 6144 + 2048 + k8 * 16 + (grp & 1) * 8);
            const float mean = st[n & 7], rd = frcp(st[8 + (n & 7)]);
            const float xv[4] = {lo16(xc[0]), hi16(xc[0]), lo16(xc[1]), hi16(xc[1])};
#pragma unroll
            for (int j = 0; j < 4; ++j) r[j] = rbf(rbf(xv[j] - mean) * rd) * rbf(1.f + rbf(r[j]));
          }
          if (n < 8 && grp < 2) st128(rs, W_H + (me >= 9 ? 18432 : 0) + img_off(n, k8) * 2 + grp * 16, u32x4{cvt2(r[0], r[1]), cvt2(r[2], r[3]), e0, e1});
          STAMP(14);
        }
        TSA(3);
      } else WAITV(DK);
      WK(2); WK(3); WK(4); WK(5);
      if (gs >= 0) BAR();
      STAMP(2);
      PFLAG();
      WK(6); WK(7);
      PACE(12); KLD(12); WAITV(DK); KLD(13); WAITV(DK);
      PACE(14); KLD(14); WAITV(DK); KLD(15); WAITV(DK);
      const int hg0 = (wave * 144 + lane) * 16, hg2 = (wave * 144 + 128 + n) * 16;
#define HREQ() do { dma_sc(ws + W_H, hg0, land); dma_sc(ws + W_H + 18432, hg0, land + 1024); dma_sc(ws + W_H + 1024, hg0, land + 2048); \
                    dma_sc(ws + W_H + 18432 + 1024, hg0, land + 3072); dma_sc(ws + W_H, hg2, land + 4096); dma_sc(ws + W_H + 18432, hg2, land + 5120); } while (0)
#define HGET() do { _Pragma("unroll") for (int i = 0; i < 6; ++i) hq[i] = *(const u32x4*)(lp + i * 1024); } while (0)
#define HQOK() __all((tag_x(hq[0], e0, e1) | tag_x(hq[1], e0, e1) | tag_x(hq[2], e0, e1) | tag_x(hq[3], e0, e1) | tag_x(hq[4], e0, e1) | tag_x(hq[5], e0, e1)) == 0u)
      PACE(16); KLD(16); WAITV(DK); KLD(17); WAITV(DK); PACE(18); KLD(18); WAITV(DK); KLD(19); WAITV(DK);
      PACE(20); KLD(20); WAITV(DK); KLD(21); WAITV(DK);
      TSA(4);
      if (qs >= 0) HREQ();
      PACE(22); FREQ(); KLD(22); WAITV(DK); KLD(23); WAITV(DK); PACE(24); KLD(24); WAITV(DK); KLD(25); WAITV(DK);
      if (qs >= 0) WAITV(4);
      TSA(5);
      PACE(26); KLD(26); KLD(27);
      if (qs >= 0) {
        u32x4 hq[6]; HGET();
        _Pragma("nounroll") for (int sp = 0; sp < SPIN_CAP && !HQOK(); ++sp) { nap(16); HREQ(); WAITV(0); HGET(); }
        TSA(6);
#define HSUM(a, b) cvt2(lo16(a) + lo16(b), hi16(a) + hi16(b))
        *(u32x2*)(lds + L_IMG + hg0 / 2) = u32x2{HSUM(hq[0][0], hq[1][0]), HSUM(hq[0][1], hq[1][1])};
        *(u32x2*)(lds + L_IMG + 512 + hg0 / 2) = u32x2{HSUM(hq[2][0], hq[3][0]), HSUM(hq[2][1], hq[3][1])};
        if (lane < 16) *(u32x2*)(lds + L_IMG + hg2 / 2) = u32x2{HSUM(hq[4][0], hq[5][0]), HSUM(hq[4][1], hq[5][1])};
      } else { WAITV(DK); PACE(28); }
      KLD(28); KLD(29);
      if (qs >= 0) {
        u32x4 wf[9], af[9];
        lds_barrier();
        slab_frags(lds, L_W, wave, lane, wf);
        img_frags(lds, L_IMG, lane, af);
        const f32x4 s = half_sum(gemv9(wf, af));
        if (n < 8 && grp < 2) *(u32x2*)(lds + L_TILE + n * 128 + wave * 16 + grp * 8) = u32x2{cvt2(s[0], s[1]), cvt2(s[2], s[3])};
      } else WAITV(DK);
      PACE(30); KLD(30); KLD(31);
      WAITV(9); FCHK();
#define MPUB(m_) do { if (wave == 0) { const u32x4 t_ = *(const u32x4*)(lds + L_TILE + lane * 16); const int ch_ = ((m_) - 18) / 9, sl_2 = (m_) - 18 - ch_ * 9; \
          st128(rs, W_M + (ch_ * 8 + (lane >> 3)) * 1152 + sl_2 * 128 + (lane & 7) * 16, u32x4{sanu(t_[0]), sanu(t_[1]), sanu(t_[2]), sanu(t_[3])}); } } while (0)
      if (qs >= 0 || me >= 18) {
        lds_barrier();
        if (qs < 0) MPUB(me);
        else if (wave < 2) {
          const u32x2 t2 = *(const u32x2*)(lds + L_TILE + tid * 8);
          st128(rs, W_Q + qs * 256 + (tid >> 4) * 16384 + n * 16, u32x4{t2[0], t2[1], e0, e1});
        }
        STAMP(15); TSA(7);
      }
      SCHED();
    }
    STAMP(3); TSB(0);
    const unsigned char* qg = ws + W_Q + (b_att * 1024 + kvh * 64) * 16;
    vo[0] = wpart + ((grp & 1) << AB[0]) + ((grp >> 1) << AB[1]) + n * 16;
    vo[1] = vo[0] + (1 << AB[2]); vo[2] = vo[0] + (1 << AB[3]); vo[3] = vo[0] + (1 << AB[2]) + (1 << AB[3]);
    TSB(1);
    PACE(32); VLD(0); WAITV(DK); VLD(1); WAITV(DK); PACE(34); VLD(2); WAITV(DK); VLD(3); WAITV(DK);
    PACE(36); VLD(4); WAITV(DK); VLD(5); WAITV(DK);
    if (wave == 1) dma_sc(qg, lane * 16, lds_base + L_B + 1024);
    PACE(38); VLD(6); WAITV(DK); VLD(7); WAITV(DK);
    PACE(40); VLD(8); WAITV(DV); VLD(9); WAITV(DV);
    if (wave == 1) dma_sc(qg, lane * 16, lds_base + L_B + 1024);
    PACE(42); VLD(10); WAITV(DV); VLD(11); WAITV(DV);
    TIE8(kb, 0); TIE8(kb, 8); TIE8(kb, 16); TIE8(kb, 24);
    PACE(44); VLD(12); VLD(13);
    u32x4 qf[2];
    {
      const u32x4* ql = (const u32x4*)(lds + L_B + 1024 + (((n >> 2) & 1) * 32 + grp * 8 + (n & 3) * 2) * 16);
      u32x4 g[2];
      _Pragma("nounroll") for (int sp = 0; sp < SPIN_CAP; ++sp) {
        g[0] = ql[0]; g[1] = ql[1];
        if (__all((tag_x(g[0], e0, e1) | tag_x(g[1], e0, e1)) == 0u)) break;
        if (wave == 1) { dma_sc(qg, lane * 16, lds_base + L_B + 1024); WAITV(0); } else nap(4);
      }
      asm volatile("" ::: "memory");
      g[0] = ql[0]; g[1] = ql[1];
      qf[0] = n < 8 ? u32x4{g[0][0], g[0][1], g[1][0], g[1][1]} : u32x4{0, 0, 0, 0};
      qf[1] = n < 8 ? u32x4{0, 0, 0, 0} : u32x4{g[0][0], g[0][1], g[1][0], g[1][1]};
    }
    STAMP(4); TSB(2);
    constexpr float SCALE = 0.088388347648318440f;
    float sc[4];
#pragma unroll
    for (int i = 0; i < 4; ++i) {
      float y[2];
#pragma unroll
      for (int t = 0; t < 2; ++t) {
        const int u = 8 * t + 2 * i;
        const f32x4 a = mfma(kb[u + 16], qf[1], mfma(kb[u], qf[0], f32x4{0.f, 0.f, 0.f, 0.f}));
        const f32x4 b = mfma(kb[u + 17], qf[1], mfma(kb[u + 1], qf[0], f32x4{0.f, 0.f, 0.f, 0.f}));
        const float own = (n & 2) ? ((n & 1) ? b[3] : a[2]) : ((n & 1) ? b[1] : a[0]), oth = (n & 2) ? ((n & 1) ? a[3] : b[2]) : ((n & 1) ? a[1] : b[0]);
        y[t] = own + dppf<0xB1>(oth);
      }
      sc[i] = ((n & 2) ? y[1] : y[0]) + dppf<0x4E>((n & 2) ? y[0] : y[1]);
      if (i == 1 || i == 3) { SCHED(); if (i == 1) VLD(14); else VLD(15); SCHED(); }
    }
#pragma unroll
    for (int i = 0; i < 4; ++i) sc[i] = rbf(rbf(sc[i]) * SCALE);
    float mx = fmaxf(fmaxf(sc[0], sc[1]), fmaxf(sc[2], sc[3]));
    mx = fmaxf(mx, dppf<0xB1>(mx)); mx = fmaxf(mx, dppf<0x4E>(mx)); mx = fmaxf(mx, dppf<0x128>(mx));
    mx = xmax32(xmax16(mx));
#pragma unroll
    for (int i = 0; i < 4; ++i) sc[i] = __expf(sc[i] - mx);
    float ls = (sc[0] + sc[1]) + (sc[2] + sc[3]);
    ls += dppf<0xB1>(ls); ls += dppf<0x4E>(ls); ls += dppf<0x128>(ls);
    ls = xsum32(xsum16(ls));
    float* stat = (float*)(lds + L_B);
    if (tid == 0) *(u32*)(lds + L_B + 512) = 0u;
    if (lane == 0 || lane == 4) *(f32x2*)(stat + (wave * 2 + (lane >> 2)) * 2) = f32x2{mx, ls};
    SCHED(); PACE(48); VLD(16); VLD(17); SCHED();
    TSB(3);
    lds_barrier();
    TSB(4);
    float M = -INFINITY, L = 0.f;
    {
      f32x2 t[8];
#pragma unroll
      for (int q = 0; q < 8; ++q) { t[q] = *(const f32x2*)(stat + (q * 2 + ((n >> 2) & 1)) * 2); M = fmaxf(M, t[q].x); }
#pragma unroll
      for (int q = 0; q < 8; ++q) L = fmaf(__expf(t[q].x - M), t[q].y, L);
    }
    if (tid < 8) st128(rs, W_ST + (w * 8 + lane) * 16, u32x4{__float_as_uint(M), __float_as_uint(L), e0, e1});
    SCHED();
#define VTR(G) do { u32x4 f_[8]; _Pragma("unroll") for (int t = 0; t < 8; ++t) _Pragma("unroll") for (int j = 0; j < 4; ++j) \
      f_[t][j] = (t & 1) ? phi(vb[(G) * 8 + 2 * j][t >> 1], vb[(G) * 8 + 2 * j + 1][t >> 1]) : plo(vb[(G) * 8 + 2 * j][t >> 1], vb[(G) * 8 + 2 * j + 1][t >> 1]); \
      _Pragma("unroll") for (int t = 0; t < 8; ++t) { vb[(G) * 8 + t] = f_[t]; asm volatile("" : "+v"(vb[(G) * 8 + t])); } SCHED(); } while (0)
    WAITV(DV);
    TIE8(vb, 0);
    VTR(0);
    if (gs >= 0) {
      const unsigned char* mb = ws + W_M + (8 + wave) * 1152;
      const unsigned ld_ = lds_base + L_W + wave * 2304;
      dma_sc(mb, lane * 16, ld_); dma_sc(mb + 9216, lane * 16, ld_ + 1024);
      if (lane < 16) dma_sc(mb + 1024, (lane >> 3) * 9216 + (lane & 7) * 16, ld_ + 2048);
    } else if (oj >= 0 && tid >= 504) {
      dma_sc(ws + W_M + op * 1152 + oj * 128, (lane & 7) * 16, lds_base + L_B + 3072 - 896);
      dma_sc((const char*)x + op * 1152 + oj * 128, (lane & 7) * 16, lds_base + L_B + 3200 - 896);
    }
    PACE(50); VLD(18); WAITV(DV); VLD(19); WAITV(DV);
    PACE(52); VLD(20); WAITV(5);
    TIE8(vb, 8);
    VTR(1);
    VLD(21); WAITV(DV);
    const int po = W_ST + ((w ^ 1) * 8 + (n & 4)) * 16;
    PACE(54);
    VLD(22); WAITV(DV); VLD(23); WAITV(DV);
    u32x4 ps;
    ald128sc(ps, ws, po);
    PACE(56);
    VLD(24); WAITV(DV); VLD(25); WAITV(DV);
    PACE(58);
    VLD(26); WAITV(DV); VLD(27); WAITV(DV);
    UNTIL(tp0 + 60 * pr_ / 4 - E60);
    VLD(28); VLD(29); WAITV(6);
    asm volatile("" : "+v"(ps));
    TIE8(vb, 16);
    UNTIL(tp0 + 62 * pr_ / 4 - E62);
    VLD(30); VLD(31);
    VTR(2);
    if (!__all(tag_ok(ps, e0, e1))) {
      int sp = 0;
      _Pragma("nounroll") do { nap(1); ps = ld128(rs, po); } while (!__all(tag_ok(ps, e0, e1)) && ++sp < SPIN_CAP);
    }
    float fac;
    {
      const float M2 = __uint_as_float(ps[0]), L2 = __uint_as_float(ps[1]);
      const float Mg = fmaxf(M, M2);
      const float Lg = __expf(M - Mg) * L + __expf(M2 - Mg) * L2;
      fac = __expf(mx - Mg) / Lg;
    }
    STAMP(1); TSB(5);
    u32x4 pf[4];
#pragma unroll
    for (int j = 0; j < 4; ++j) {
      const float p_ = sc[j] * fac;
      pf[0][j] = cvt2(p_, dppf<0xB1>(p_));
      pf[1][j] = dppu<0x102>(pf[0][j]); pf[2][j] = dppu<0x108>(pf[0][j]); pf[3][j] = dppu<0x10A>(pf[0][j]);
    }
    f32x4 oa[8];
#pragma unroll
    for (int t = 0; t < 8; ++t) oa[t] = f32x4{0.f, 0.f, 0.f, 0.f};
    SCHED();
#pragma unroll
    for (int G = 0; G < 2; ++G)
#pragma unroll
      for (int t = 0; t < 8; ++t) oa[t] = mfma(pf[G], vb[G * 8 + t], oa[t]);
    SCHED();
#pragma unroll
    for (int t = 0; t < 8; ++t) oa[t] = mfma(pf[2], vb[16 + t], oa[t]);
    SCHED();
    int t2_ = threadIdx.x; asm volatile("" : "+v"(t2_));
    const int tid = t2_, lane = tid & 63, n = lane & 15, grp = lane >> 4;
    int oo = W_O + ((kvh * 8 + b_att) * 2 + s_att) * 1024 + lane * 16;
    int mw_ = L_C + wave * 1024 + lane * 32, mr_ = L_C + (grp * 16 + (lane & 7) * 2 + (n >> 3)) * 16;
    asm volatile("" : "+v"(oo), "+v"(mw_), "+v"(mr_), "+v"(pf[3]));
    u32x4 mg[2]; u32 mh[2];
    const u32 dv = (lane >> 3) * (gs >= 0 ? 128 : 1152) + (((lane & 7) ^ (lane >> 4)) << 4);
    const char* db = gs >= 0 ? (const char*)(wd + (size_t)gs * 64 * D) + wave * 1024 : (const char*)(wo + (size_t)(op * 512 + wave * 64) * D + oj * 64);
    const int NU = min(max(112 * H1 + 78 * (H2 + H3) - 576, 0), 572), NS = (2 * (576 - NU) + 111) / 112;
#define HROW(i_) (((i_) < NU ? (const char*)wu + (size_t)(576 - NU) * 9216 : (const char*)wg - (size_t)NU * 9216) + (size_t)max(min(i_, NU + 575), 0) * 9216 + xcd * 1152)
    const char* ha;
    if (gs >= 0) {
#pragma unroll
      for (int i = 0; i < 2; ++i) { mg[i] = *(const u32x4*)(lds + L_W + wave * 2304 + i * 1024 + lane * 16); mh[i] = *(const u32*)(lds + L_W + wave * 2304 + 2048 + i * 128 + (lane >> 1) * 4); }
      lds_barrier();
#pragma unroll
      for (int j = 0; j < 4; ++j) dma(db + j * 8192, dv, lds_base + L_W + 73728 + (j * 8 + wave) * 1024);
      WAITV(6);
    } else if (oj >= 0) {
      BAR();
#pragma unroll
      for (int j = 0; j < NB; ++j) dma(db + j * 8 * 1152, dv ^ ((j & 1) * 64), lds_base + L_W + (wave * 8 + j) * 1024);
      asm volatile("s_waitcnt vmcnt(%0)" :: "n"(NB + 2) : "memory"); SCHED();
    } else {
      const int r_ = max(min(lane / 9, H1 - 1), 0), c_ = lane - (lane / 9) * 9;
      ha = HROW(r_ * 112 + idx * 8 + wave) + c_ * 128;
      if (lane >= H1 * 9) {
        const int k_ = min((idx * 8 + wave) * NS + min(lane - H1 * 9, NS - 1), 2 * (576 - NU) - 1), g0 = ((3 - (int)((uintptr_t)wu >> 7) - xcd * 9) & 7) + (k_ & 1) * 8;
        ha = (const char*)wu + (size_t)(k_ >> 1) * 9216 + (xcd * 9 + (g0 < 9 ? g0 : g0 - 8)) * 128;
      }
      asm volatile("" : "+v"(ha));
      WAITV(2);
    }
    u32x4 vf[8];
    asm volatile("s_nop 0 ; both branches above cover V up to the last two" : "+v"(vb[24]), "+v"(vb[25]), "+v"(vb[26]), "+v"(vb[27]), "+v"(vb[28]), "+v"(vb[29]));
#pragma unroll
    for (int t = 0; t < 8; ++t)
#pragma unroll
      for (int j = 0; j < 3; ++j) vf[t][j] = (t & 1) ? phi(vb[24 + 2 * j][t >> 1], vb[25 + 2 * j][t >> 1]) : plo(vb[24 + 2 * j][t >> 1], vb[25 + 2 * j][t >> 1]);
#pragma unroll
    for (int t = 0; t < 8; ++t) asm volatile("" : "+v"(vf[t][0]), "+v"(vf[t][1]), "+v"(vf[t][2]));
    if (gs >= 0) WAITV(4); else if (oj >= 0) WAITV(NB); else { WAITV(0); dma_keep64(ha, lds_base + L_B + 4096 + wave * 256); }
    asm volatile("s_nop 0 ; both branches above cover V" : "+v"(vb[30]), "+v"(vb[31]));
#pragma unroll
    for (int t = 0; t < 8; ++t) {
      vf[t][3] = (t & 1) ? phi(vb[30][t >> 1], vb[31][t >> 1]) : plo(vb[30][t >> 1], vb[31][t >> 1]);
      oa[t] = mfma(pf[3], vf[t], oa[t]);
    }
    STAMP(5); TSB(6);
    if (grp < 2) {
      *(f32x4*)(lds + mw_) = f32x4{oa[0][0], oa[1][0], oa[2][0], oa[3][0]};
      *(f32x4*)(lds + mw_ + 16) = f32x4{oa[4][0], oa[5][0], oa[6][0], oa[7][0]};
    }
    lds_barrier();
    if (wave == 0) {
      f32x4 m[8];
#pragma unroll
      for (int q = 0; q < 8; ++q) m[q] = *(const f32x4*)(lds + mr_ + q * 1024);
      SCHED();
      const f32x4 o = ((m[0] + m[1]) + (m[2] + m[3])) + ((m[4] + m[5]) + (m[6] + m[7]));
      st128(rs, oo, u32x4{sanf(o[0]), sanf(o[1]), sanf(o[2]), sanf(o[3])});
    }
    STAMP(6); TSB(7); SCHED();
  if (oj >= 0) {
    const u32 t6o_ = (u32)__builtin_amdgcn_s_memrealtime();
    int wa = L_W + (wave >> 2) * 32768 + (16 * (grp >> 1) + 8 * (n >> 3) + 4 * (grp & 1) + ((n >> 2) & 1)) * 128 + (((2 * (wave & 3) + ((lane >> 1) & 1)) ^ (4 * (n >> 3) + 2 * (grp & 1))) << 4) + (lane & 1) * 8;
    int iw = L_OI + wave * 1024 + (((lane >> 1) & 3) * 16 + ((4 * grp + 2 * (lane & 1) + (n >> 3)) ^ ((lane >> 1) & 1) ^ (lane & 4))) * 16;
    int ir = L_OI + (wave >> 2) * 4096 + (grp * 16 + (n ^ (grp & 1) ^ (4 * (grp >> 1)))) * 16, ix = (lane & 2) ? 0xeeee : 0x4444;
    int rw = L_OI + 8192 + (wave >> 2) * 2304 + grp * 576 + (wave & 3) * 64 + n * 4, rr = L_OI + 8192 + wave * 288 + ((n & 7) * 2 + (n >> 3)) * 16;
    int xo = W_XP + (wave * 9 + oj) * 2048 + (n >> 3) * 1024 + op * 128 + (n & 7) * 16;
    int ob = W_O + ((op * 2 + (wave >> 2)) * 8 + (lane >> 3)) * 2048 + (wave & 3) * 256 + (lane & 7) * 16;
    asm volatile("" : "+v"(rw), "+v"(rr), "+v"(xo), "+v"(ob), "+v"(iw), "+v"(ir), "+v"(wa), "+v"(ix));
    float xv[2], ga[2]; int xi, xq;
    if (wave == 7) {
      const int g = lane & 7;
      u32 mm[2];
#pragma unroll
      for (int i = 0; i < 2; ++i) {
        xv[i] = b2f(*(const u16*)(lds + L_B + 3200 + g * 16 + i * 8 + grp * 2));
        mm[i] = *(const u32*)(lds + L_B + 3072 + g * 16 + i * 8 + (grp >> 1) * 4);
      }
      _Pragma("nounroll") for (int sp = 0; sp < SPIN_CAP && !__all(mxu(mm[0], mm[1]) != 0xffffffffu); ++sp) {
        nap(4);
#pragma unroll
        for (int i = 0; i < 2; ++i) mm[i] = ld32(rs, W_M + op * 1152 + oj * 128 + g * 16 + i * 8 + (grp >> 1) * 4);
      }
      ga[0] = pick2(mm[0], grp); ga[1] = pick2(mm[1], grp);
      xi = W_XP + (op * 9 + oj) * 2048 + lane * 16; xq = W_X1 + ((lane >> 3) * 8 + op) * 1152 + oj * 128 + g * 16;
      asm volatile("" : "+v"(xv[0]), "+v"(xv[1]), "+v"(ga[0]), "+v"(ga[1]), "+v"(xi), "+v"(xq));
    }
    u32x4 a[4];
    UNTIL(t6o_ + OD);
#define OLD() do { _Pragma("unroll") for (int p = 0; p < 4; ++p) a[p] = ld128(rs, ob + (p >> 1) * 1024 + (p & 1) * 128); } while (0)
    OLD();
    SCHED();
    WAITV(4); BAR();
    TSC(0);
    u32x8 bw[4];
    f32x4 acc = {0.f, 0.f, 0.f, 0.f};
#define WFRAG(s0_, s1_) do { _Pragma("unroll") for (int s = s0_; s < s1_; ++s) { \
      const u32x2 t0 = trd(lds, wa + s * 8192), t1 = trd(lds, (wa ^ 16) + 256 + s * 8192), t2 = trd(lds, wa + 4096 + s * 8192), t3 = trd(lds, (wa ^ 16) + 4352 + s * 8192); \
      bw[s] = u32x8{t0[0], t0[1], t1[0], t1[1], t2[0], t2[1], t3[0], t3[1]}; } } while (0)
    WFRAG(0, 4);
    asm volatile("" : "+v"(bw[0]), "+v"(bw[1]), "+v"(bw[2]), "+v"(bw[3]), "+v"(acc));
    asm volatile("" : "+v"(a[0]), "+v"(a[1]), "+v"(a[2]), "+v"(a[3]));
    _Pragma("nounroll") for (int sp = 0; sp < SPIN_CAP; ++sp) {
      const f32x4 s0 = asf4(a[0]) + asf4(a[2]), s1 = asf4(a[1]) + asf4(a[3]);
      *(u32x4*)(lds + iw) = u32x4{cvt2(s0[0], s0[1]), cvt2(s0[2], s0[3]), cvt2(s1[0], s1[1]), cvt2(s1[2], s1[3])};
      SCHED();
      if (!__any(mxu(mxu(mx4(a[0]), mx4(a[1])), mxu(mx4(a[2]), mx4(a[3]))) == 0xffffffffu)) break;
      nap(1);
      OLD();
    }
    STAMP(7); TSC(1);
    lds_barrier();
    TSC(2);
    u32x4 of[4];
#pragma unroll
    for (int k = 0; k < 4; ++k) { of[k] = *(const u32x4*)(lds + ir + k * 1024); SCHED(); }
#pragma unroll
    for (int k = 0; k < 4; ++k) acc = smf(of[k], bw[k], acc, ix);
    const f32x2 e = smf_sum(acc);
    *(float*)(lds + rw) = e.x; *(float*)(lds + rw + 288) = e.y;
    lds_barrier();
    TSC(3);
    if (lane < 16) {
      const f32x4 r = *(const f32x4*)(lds + rr) + *(const f32x4*)(lds + rr + 2304);
      st128(rs, xo, u32x4{sanf(r[0]), sanf(r[1]), sanf(r[2]), sanf(r[3])});
    }
    TSC(4);
    if (wave == 7) {
      UNTIL((u32)__builtin_amdgcn_s_memrealtime() + RD);
      u32 xw[8], t_; int sp = SPIN_CAP;
      asm volatile(".LBBp1_%=:\n\tbuffer_load_dwordx4 v[196:199], %10, %11, 0 offen sc1\n\tbuffer_load_dwordx4 v[200:203], %10, %11, 0 offen offset:1024 sc1\n\t"
                   "s_sub_u32 %9, %9, 1\n\ts_waitcnt vmcnt(1)\n\tv_max3_u32 %8, v196, v197, v198\n\tv_max_u32 %8, %8, v199\n\ts_waitcnt vmcnt(0)\n\t"
                   "v_max3_u32 %8, %8, v200, v201\n\tv_max3_u32 %8, %8, v202, v203\n\tv_cmp_eq_u32 vcc, -1, %8\n\ts_cbranch_scc1 .LBBp2_%=\n\ts_cbranch_vccnz .LBBp1_%=\n.LBBp2_%=:"
                   : "=&{v196}"(xw[0]), "=&{v197}"(xw[1]), "=&{v198}"(xw[2]), "=&{v199}"(xw[3]), "=&{v200}"(xw[4]), "=&{v201}"(xw[5]), "=&{v202}"(xw[6]), "=&{v203}"(xw[7]),
                     "=&v"(t_), "+s"(sp) : "v"(xi), "s"(rs) : "vcc", "scc", "memory");
      const u32x4 xp[2] = {{xw[0], xw[1], xw[2], xw[3]}, {xw[4], xw[5], xw[6], xw[7]}};
      TSC(5);
      const u32x2 r0 = gather4(rbf(xv[0] + rbf(ga[0] * rbf(red8(asf4(xp[0])))))), r1 = gather4(rbf(xv[1] + rbf(ga[1] * rbf(red8(asf4(xp[1]))))));
      const u32x4 xr = {sanu(r0[0]), sanu(r0[1]), sanu(r1[0]), sanu(r1[1])};
      st128(rs, xq, xr);
      SCHED();
      if (lane < 9) st128(rs, lane < 8 ? xq + 73728 : W_TS + (oj * 8 + op) * 128, lane < 8 ? xr : u32x4{(u32)__builtin_amdgcn_s_memrealtime(), 0u, e0, e1});
      STAMPW(8); TSC(6);
    }
  }
  if (oj >= 0) STAMP(13);
  if (gs >= 0) {
    constexpr int L_H2 = L_B;
    constexpr int L_ACT = L_C + 4608;
    const int b = wave;
    u32x8 bw[9], wdf[5];
    f32x4 d[6];
    const u32 gv = (lane >> 3) * (FF * 2) + (((lane & 7) ^ ((wave & 1) * 4 + (lane >> 4))) << 4);
#define GDMA(j0, j1) do { _Pragma("unroll") for (int j = j0; j < j1; ++j) dma((const char*)(wg + gs * 64) + (size_t)(j * 64 + wave * 8) * (FF * 2), gv, lds_base + L_W + 73728 + (j * 8 + wave) * 1024); } while (0)
#define UDMA(j0, j1) do { _Pragma("unroll") for (int j = j0; j < j1; ++j) dma((const char*)(wu + gs * 64) + (size_t)(j * 64 + wave * 8) * (FF * 2), gv ^ 16u, lds_base + L_W + (j * 8 + wave) * 1024); } while (0)
    BAR(); SCHED();
    TSC(0);
    float sb[9], cb1[9];
    int ia[2], ra, wa, ix, yo[3], tw, tr_, xa, xb;
    u32x4 x8, fy; u32 x1;
    asm volatile("" : "=v"(fy));
    int fo = W_FY + min(lane, NPY / 2 - 1) * 16;
    {
#pragma unroll
      for (int j = 4; j < 9; ++j) dma(db + j * 8192, dv, lds_base + L_W + 73728 + (j * 8 + wave) * 1024);
      BAR();
      UDMA(0, UA);
    ia[0] = L_H2 + (lane >> 3) * 1024 + (((lane >> 1) & 3) * 16 + ((4 * (b >> 1) + 2 * (lane & 1) + (b & 1)) ^ ((lane >> 1) & 1) ^ (lane & 4))) * 16;
    ia[1] = L_H2 + 8192 + (grp * 16 + ((4 * (b >> 1) + 2 * (n >> 3) + (b & 1)) ^ (grp & 1) ^ (4 * (grp >> 1)))) * 16 + (lane & 7) * 2;
    ra = L_H2 + (grp * 16 + (n ^ (grp & 1) ^ (4 * (grp >> 1)))) * 16;
    wa = L_W + ((lane & 2) ? 0 : 73728) + (16 * (grp >> 1) + 8 * (n >> 3) + 4 * (grp & 1) + ((n >> 2) & 1)) * 128 + ((wave ^ (4 * (n >> 3) + 2 * (grp & 1)) ^ ((lane >> 1) & 1)) << 4) + (lane & 1) * 8;
    ix = (lane & 2) ? 0xeeee : 0x4444;
    tw = L_ACT + ((wave >> 1) * 16 + 4 * grp + (n & 2) + (n >> 3)) * 16 + ((n >> 2) & 1) * 8 + (wave & 1) * 4 + (n & 1) * 2; tr_ = L_ACT + lane * 16;
    yo[0] = W_Y + (((grp * 2) * 18 + wave * 2 + (n >> 3)) * NG + gs) * 128 + (n & 7) * 16;
    yo[1] = W_Y + ((grp * 2 * 18 + 16 + ((wave >> 1) & 1)) * NG + gs) * 128 + (wave & 1) * 64 + n * 4;
    yo[2] = yo[0] + 18 * NG * 128;
    xa = W_X1 + (xcd * 8 + b) * 1152 + lane * 16; xb = W_X1 + (xcd * 8 + b) * 1152 + 1024 + (lane >> 1) * 4;
    asm volatile("" : "+v"(ia[0]), "+v"(ia[1]), "+v"(ra), "+v"(wa), "+v"(ix), "+v"(tw), "+v"(tr_), "+v"(yo[0]), "+v"(yo[1]), "+v"(yo[2]), "+v"(xa), "+v"(xb), "+v"(fo));
#define MOK() __all(mxu(mxu(mx4(mg[0]), mx4(mg[1])), mxu(mh[0], mh[1])) != 0xffffffffu)
#define MLD(b_) do { _Pragma("unroll") for (int i = 0; i < 2; ++i) { mg[i] = ld128(rs, W_M + (8 + i * 8 + (b_)) * 1152 + lane * 16); mh[i] = ld32(rs, W_M + (8 + i * 8 + (b_)) * 1152 + 1024 + (lane >> 1) * 4); } } while (0)
#define MUNP() do { _Pragma("unroll") for (int i = 0; i < 4; ++i) { sb[2 * i] = lo16(mg[0][i]); sb[2 * i + 1] = hi16(mg[0][i]); \
                      cb1[2 * i] = rbf(1.f + lo16(mg[1][i])); cb1[2 * i + 1] = rbf(1.f + hi16(mg[1][i])); } \
                    sb[8] = pick2(mh[0], lane); cb1[8] = rbf(1.f + pick2(mh[1], lane)); \
                    _Pragma("unroll") for (int i = 0; i < 9; ++i) asm volatile("" : "+v"(sb[i]), "+v"(cb1[i])); } while (0)
      if (!MOK()) {
        int sp = 0;
        _Pragma("nounroll") do { nap(2); MLD(b); } while (!MOK() && ++sp < SPIN_CAP);
      }
      MUNP();
      WAITV(UA); BAR();
      {
        const int q = n >> 2, p = lane & 3, r = 16 * (grp >> 1) + 4 * (grp & 1) + q;
        const int a0 = L_W + 73728 + r * 1152 + (p & 1) * 8;
#pragma unroll
        for (int t = 0; t < 5; ++t) {
          const int a = t < 4 ? a0 + wave * 128 + (((t + (p >> 1) * 4) ^ (((r + wave) >> 1) & 3)) << 4) : a0 + 1024 + ((((wave & 3) * 2 + (p >> 1)) ^ ((r >> 1) & 3)) << 4);
          const u32x2 t0 = trd(lds, a), t1 = trd(lds, a + 9216), t2 = trd(lds, a + 36864), t3 = trd(lds, a + 36864 + 9216);
          wdf[t] = u32x8{t0[0], t0[1], t1[0], t1[1], t2[0], t2[1], t3[0], t3[1]};
        }
#pragma unroll
        for (int t = 0; t < 5; ++t) asm volatile("" : "+v"(wdf[t]));
      }
      TSC(1);
      UDMA(UA, 9);
      lds_barrier();
      GDMA(0, 9);
      WAITV(4); BAR();
      WFRAG(0, 5);
      asm volatile("" : "+v"(bw[0]), "+v"(bw[1]), "+v"(bw[2]), "+v"(bw[3]), "+v"(bw[4]));
#pragma unroll
      for (int t = 0; t < 6; ++t) { d[t] = f32x4{0.f, 0.f, 0.f, 0.f}; asm volatile("" : "+v"(d[t])); }
      TSC(2);
      WAITV(0);
      asm volatile("buffer_load_dwordx4 v[196:199], %2, %4, 0 offen sc1\n\tbuffer_load_dword v200, %3, %4, 0 offen sc1"
                   : "=&{v[196:199]}"(x8), "=&{v200}"(x1) : "v"(xa), "v"(xb), "s"(rs) : "memory");
      BAR();
      WFRAG(5, 9);
      SCHED();
      STAMP(13);
    }
    {
      TSC(3);
#define XOK() __all(mxu(mx4(x8), x1) != 0xffffffffu)
      u32 t_; int sp = SPIN_CAP;
      asm volatile("s_branch .LBBp4_%=\n.LBBp3_%=:\n\tbuffer_load_dwordx4 v[196:199], %4, %6, 0 offen sc1\n\tbuffer_load_dword v200, %5, %6, 0 offen sc1\n.LBBp4_%=:\n\t"
                   "s_sub_u32 %3, %3, 1\n\ts_waitcnt vmcnt(1)\n\tv_max3_u32 %2, v196, v197, v198\n\ts_waitcnt vmcnt(0)\n\t"
                   "v_max3_u32 %2, %2, v199, v200\n\tv_cmp_eq_u32 vcc, -1, %2\n\ts_cbranch_scc1 .LBBp5_%=\n\ts_cbranch_vccnz .LBBp3_%=\n.LBBp5_%=:"
                   : "+{v[196:199]}"(x8), "+{v200}"(x1), "=&v"(t_), "+s"(sp) : "v"(xa), "v"(xb), "s"(rs) : "vcc", "scc", "memory");
      STAMP(9); TSC(4);
      if (wave == 0) fy = ld128(rs, fo);
      SCHED();
      u32x4 h8; u32 h1;
      ln9f(x8, pick2(x1, lane), cb1, sb, h8, h1);
      *(u32x4*)(lds + ia[0]) = h8;
      *(u16*)(lds + ia[1]) = (u16)h1;
      TSC(5);
      lds_barrier();
      u32x4 af[9];
#pragma unroll
      for (int s = 0; s < 9; ++s) { af[s] = *(const u32x4*)(lds + ra + s * 1024); SCHED(); }
      f32x4 acc = d[5];
#pragma unroll
      for (int s = 0; s < 9; ++s) acc = smf(af[s], bw[s], acc, ix);
      const f32x2 vv = smf_sum(acc);
      const int v0 = __float_as_int(vv.x), v1 = __float_as_int(vv.y);
      const float g = __int_as_float(__builtin_amdgcn_update_dpp(v0, v1, 0x128, 0xf, 0xc, false)), u = __int_as_float(__builtin_amdgcn_update_dpp(v1, v0, 0x128, 0xf, 0x3, false));
      const float gr = rbf(g);
      *(u16*)(lds + tw) = (u16)cvt2(rbf(gr * frcp(1.f + __expf(-gr))) * rbf(u), 0.f);
      SCHED();
      if (wave == 0 && !FOK(fy)) {
        int sp = 0;
        _Pragma("nounroll") do { nap(2); fy = ld128(rs, fo); } while (!FOK(fy) && ++sp < SPIN_CAP);
      }
    }
    lds_barrier();
    STAMP(10); TSC(6);
    {
      const u32x4 af = *(const u32x4*)(lds + tr_);
#pragma unroll
      for (int t = 0; t < 4; ++t) d[t] = smf(af, wdf[t], d[t], ix);
      SCHED();
      f32x2 e[5];
#pragma unroll
      for (int t = 0; t < 4; ++t) e[t] = smf_sum(d[t]);
#pragma unroll
      for (int j = 0; j < 2; ++j)
        st128(rs, yo[2 * j], u32x4{sanf(e[0][j]), sanf(e[1][j]), sanf(e[2][j]), sanf(e[3][j])});
      SCHED();
      if (wave < 4) {
        e[4] = smf_sum(smf(af, wdf[4], d[4], ix));
        __builtin_amdgcn_raw_buffer_store_b32(sanf(e[4][0]), rs, yo[1], 0, 16);
        __builtin_amdgcn_raw_buffer_store_b32(sanf(e[4][1]), rs, yo[1], 18 * NG * 128, 16);
      }
      TSC(7);
    }
  }
  {
  int t3_ = threadIdx.x; asm volatile("" : "+v"(t3_));
  const int tid = t3_, lane = tid & 63, n = lane & 15, grp = lane >> 4;
  const u32 t6_ = (u32)__builtin_amdgcn_s_memrealtime();
  if (idx >= 1 && idx < 14 && wave >= 2) {
    const int r_ = min(lane / 9, H2 - 1), c_ = lane - (lane / 9) * 9;
    ha = HROW(112 * H1 + ((wave - 2) * 13 + idx - 1) * H2 + r_) + c_ * 128;
    asm volatile("" : "+v"(ha));
    UNTIL(t6_ + HD2 + (wave - 2) * HS2);
    TSC(0);
    {
      const rsrc_t ry = __builtin_amdgcn_make_buffer_rsrc(ws + W_Y, 0, 144 * NG * 128, 0x00020000);
      const u32x4 ones = {0xffffffffu, 0xffffffffu, 0xffffffffu, 0xffffffffu};
      const int yp = (w - 8) * 13312 + (tid - 128) * 16;
      if (wave == 2) st128(ry, yp + 12288, ones);
      st128(ry, yp, ones); st128(ry, yp + 6144, ones);
      SCHED();
    }
    dma_keep64(ha, lds_base + L_B + 4096 + wave * 256);
    TSC(1);
    {
      ha = HROW(112 * H1 + 78 * H2 + ((wave - 2) * 13 + idx - 1) * H3 + min(lane / 9, H3 - 1)) + c_ * 128;
      asm volatile("" : "+v"(ha));
      UNTIL(t6_ + HD3 + (wave - 2) * HS2);
      dma_keep64(ha, lds_base + L_B + 4096 + wave * 256);
    }
    WAITV(2);
    if (lane == 0 && __hip_atomic_fetch_add((__attribute__((address_space(3))) u32*)((lds_t*)lds + L_B + 512), 1u, __ATOMIC_RELAXED, __HIP_MEMORY_SCOPE_WORKGROUP) == 5u)
      st64(rs, W_FY + (w - 8) * 8, u32x2{e0, e1});
  }
  if (ml >= 0) {
    BAR();
    slab_dma(w_mod + ml * 64, NMOD * 2, lds_base + L_W, wave, lane);
    u32x4 wf[9], af[9];
    img_frags(lds, L_IMG, lane, af);
    asm volatile("s_waitcnt vmcnt(0)\n\ts_barrier" ::: "memory");
    slab_frags(lds, L_W, wave, lane, wf);
    const f32x4 s = half_sum(gemv9(wf, af));
    if (n < 8 && grp < 2) *(u32x2*)(lds + L_TILE + n * 128 + wave * 16 + grp * 8) = u32x2{cvt2(s[0], s[1]), cvt2(s[2], s[3])};
    lds_barrier();
    MPUB(ml);
  }

  if (idx >= 5 && idx < 23 && wave == 1) {
    const int b = xcd, jh = idx - 5, col = jh * 32 + (jh < 16 ? (lane & 7) + grp * 8 : (lane & 7) * 4 + grp);
    const rsrc_t rw_ = __builtin_amdgcn_make_buffer_rsrc((void*)w_r, 0, D * 2, 0x00020000);
    const u32x4 wr8 = __builtin_amdgcn_raw_buffer_load_b128(rw_, lane * 16, 0, 0);
    const float wr1 = b2f(__builtin_amdgcn_raw_buffer_load_b16(rw_, 1024 + lane * 2, 0, 0));
    u32x4 mg[2]; u32 mh[2];
    _Pragma("nounroll") for (int sp = 0; sp < SPIN_CAP; ++sp) { MLD(b); if (MOK()) break; nap(4); }
    float sb[9], cb1[9];
    MUNP();
    UNTIL(tstart + TX + 64 * PR / 4 + FSL);
    const int xa = W_X1 + (64 + b) * 1152 + lane * 16, xb = W_X1 + (64 + b) * 1152 + 1024 + (lane >> 1) * 4;
    u32x4 x8; u32 x1;
    _Pragma("nounroll") for (int sp = 0; sp < SPIN_CAP; ++sp) { x8 = ld128(rs, xa); x1 = ld32(rs, xb); if (XOK()) break; nap(1); }
    const u32 tx_ = (u32)__builtin_amdgcn_s_memrealtime();
    const int go = W_M + (24 + b) * 1152 + (col >> 1) * 4, fo = W_FY + min(lane, NPY / 2 - 1) * 16;
    u32 gq = ld32(rs, go); u32x4 fy = ld128(rs, fo);
    u32x4 tq = ld128(rs, W_TS + lane * 128);
    SCHED();
    const float v8 = pick2(x1, lane);
    u32x4 h8; u32 h1;
    ln9(x8, v8, cb1, sb, h8, h1);
    unsigned char* row = lds + L_C;
    *(u32x4*)(row + lane * 16) = x8; *(u32x4*)(row + 1152 + lane * 16) = h8;
    *(u16*)(row + 1024 + lane * 2) = (u16)(__float_as_uint(v8) >> 16); *(u16*)(row + 2176 + lane * 2) = (u16)h1;
    float r = lo16(h1) * wr1;
#pragma unroll
    for (int i = 0; i < 4; ++i) r = fmaf(hi16(h8[i]), hi16(wr8[i]), fmaf(lo16(h8[i]), lo16(wr8[i]), r));
    r = rbf(wsum(r));
    float rsel = rbf(1.f / (1.f + __expf(-r)));
    float x1v = b2f(*(const u16*)(row + col * 2)), h2 = b2f(*(const u16*)(row + 1152 + col * 2));
    asm volatile("" : "+v"(rsel), "+v"(x1v), "+v"(h2));
    asm volatile("" : "+v"(tq));
    _Pragma("nounroll") for (int sp = 0; sp < SPIN_CAP && !(__all(gq != 0xffffffffu) && FOK(fy)); ++sp) { nap(2); gq = ld32(rs, go); fy = ld128(rs, fo); }
    float gbv = pick2(gq, col);
    u32 tgo;
    {
      int d_ = tag_ok(tq, e0, e1) ? (int)(tq[0] - tx_) : 0;
      d_ = max(d_, (int)dppu<0xB1>(d_)); d_ = max(d_, (int)dppu<0x4E>(d_)); d_ = max(d_, (int)dppu<0x141>(d_)); d_ = max(d_, (int)dppu<0x140>(d_));
      d_ = max(max(__builtin_amdgcn_readlane(d_, 0), __builtin_amdgcn_readlane(d_, 16)), max(__builtin_amdgcn_readlane(d_, 32), __builtin_amdgcn_readlane(d_, 48)));
      tgo = tx_ + min(d_ + TGO, TGO - 15);
    }
    int yi = W_Y + (b * 18 + jh) * NG * 128 + lane * 16, oo = (lane & 8) ? 0x10000 : (b * D + col) * 2;
    rsrc_t ro = __builtin_amdgcn_make_buffer_rsrc(out, 0, 8 * D * 2, 0x00020000);
    asm volatile("" : "+s"(ro));
    asm volatile("" : "+v"(rsel), "+v"(x1v), "+v"(h2), "+v"(gbv), "+v"(yi), "+v"(oo));
    TSC(5);
    UNTIL(tgo);
    _Pragma("nounroll") for (int sp = 1; sp <= SPIN_CAP; ++sp) {
      u32x4 y[9];
#pragma unroll
      for (int i = 0; i < 9; ++i) y[i] = __builtin_amdgcn_raw_buffer_load_b128(rs, yi + (i & 3) * 1024, (i >> 2) * 4096, 16);
      const float z = red8((((asf4(y[0]) + asf4(y[1])) + (asf4(y[2]) + asf4(y[3]))) + ((asf4(y[4]) + asf4(y[5])) + (asf4(y[6]) + asf4(y[7])))) + asf4(y[8]));
      const float res = x1v + rbf(gbv * rbf(h2 + rbf(rbf(z) * rsel)));
      __builtin_amdgcn_raw_buffer_store_b16((u16)(cvt2(res, 0.f) & 0xffffu), ro, oo, 0, OUTAUX);
      SCHED();
      if (!__any(z != z)) break;
      if ((sp & 7) == 0) {
        u32 m = mx4(y[8]);
#pragma unroll
        for (int i = 0; i < 8; ++i) m = mxu(m, mx4(y[i]));
        if (!__any(m == 0xffffffffu)) break;
      }
    }
    STAMPW(12); TSC(7);
  }
  TSOUT();
  }
  }
}

extern "C" void kernel_launch(void* const* d_in, const int* in_sizes, int n_in, void* d_out, int out_size, void* d_ws, size_t ws_size,
                              hipStream_t stream) {
  fused_kernel<<<NWG, NT, 0, stream>>>((const u16*)d_in[0], (const u16*)d_in[1], (const u16*)d_in[2], (const u16*)d_in[3], (const u16*)d_in[4],
                                       (const u16*)d_in[6], (const u16*)d_in[7], (const u16*)d_in[8], (const u16*)d_in[9], (const u16*)d_in[10],
                                       (const u16*)d_in[11], (unsigned char*)d_ws, (u16*)d_out);
}
```
